```python
import torch

def _softmax(x, axis=-1):
    return torch.softmax(x, dim=axis)
def _gelu(x):
    return torch.nn.functional.gelu(x, approximate="tanh")
def _silu(x):
    return torch.nn.functional.silu(x)
def _sigmoid(x):
    return torch.sigmoid(x)
def _layernorm(x, axis=-1, eps=1e-5):
    m = x.mean(dim=axis, keepdim=True)
    v = x.var(dim=axis, keepdim=True, unbiased=False)
    return (x - m) / torch.sqrt(v + eps)
def _scatter_add(tgt, idx, src, axis=0):
    return tgt.clone().index_add_(axis, idx, src)
def _segment_sum(src, seg, n):
    out = torch.zeros((n,) + src.shape[1:], dtype=src.dtype)
    return out.index_add_(0, seg, src)
def _pad(x, pw):
    flat = tuple(v for pair in reversed(pw) for v in pair)
    return torch.nn.functional.pad(x, flat)

kernel_name = 'gated_mix__sliding_local_dilated__dense_activation_gelu__prompt'

def setup_inputs():

    _w = (lambda a: (lambda t: t.to(torch.bfloat16) if t.is_floating_point() else t)(torch.as_tensor(a)))
    def _A_setup():
        B, S, D = 1, 2048, 640
        return {
            "x": _w((lambda *s: torch.randn(*s, dtype=torch.bfloat16) * 0.05)(B, S, D)),
            "wqkv": _w((lambda *s: torch.randn(*s, dtype=torch.bfloat16) * 0.8)(D, 3 * D)),
            "wo": _w((lambda *s: torch.randn(*s, dtype=torch.bfloat16) * 0.05)(D, D)),
        }
    _A_in = _A_setup()

    def _B_setup():
        B, S, D, F = 1, 2048, 640, 1536
        return {
            "x": _w((lambda *s: torch.randn(*s, dtype=torch.bfloat16) * 0.05)(B, S, D)),
            "w1": _w((lambda *s: torch.randn(*s, dtype=torch.bfloat16) * 12.8)(D, F) * (D ** -0.5)),
            "b1": _w((lambda *s: torch.randn(*s, dtype=torch.bfloat16) * 12.8)(F) * 0.01),
            "w2": _w((lambda *s: torch.randn(*s, dtype=torch.bfloat16) * 1.6)(F, D) * (F ** -0.5)),
            "b2": _w((lambda *s: torch.randn(*s, dtype=torch.bfloat16) * 12.8)(D) * 0.01),
        }
    _B_in = _B_setup()

    return {
        "A_x": _A_in["x"],
        "A_wqkv": _A_in["wqkv"],
        "A_wo": _A_in["wo"],
        "B_x": _B_in["x"],
        "B_w1": _B_in["w1"],
        "B_b1": _B_in["b1"],
        "B_w2": _B_in["w2"],
        "B_b2": _B_in["b2"],
        "gate_w": _w((lambda *s: torch.randn(*s, dtype=torch.bfloat16) * 0.05)(640) * 16.0),
    }

def reference(A_x, A_wqkv, A_wo, B_x, B_w1, B_b1, B_w2, B_b2, gate_w):
    def _A_ref(x, wqkv, wo):
        B, S, D = x.shape
        H = 32; Dh = D // H
        W, dil = 8, 2
        qkv = (lambda x, *d: x.permute(*d))((x @ wqkv).reshape(B, S, 3, H, Dh), 2, 0, 3, 1, 4)
        q, k, v = qkv[0], qkv[1], qkv[2]
        scale = Dh ** -0.5
        pos = torch.arange(S)
        off = torch.arange(W) * dil
        idx = pos[:, None] - off[None, :]
        mask = idx >= 0
        idx_c = torch.where(mask, idx, 0).reshape(-1)
        kg = k[:, :, idx_c, :].reshape(B, H, S, W, Dh)
        vg = v[:, :, idx_c, :].reshape(B, H, S, W, Dh)
        logits = torch.einsum("bhsd,bhswd->bhsw", q, kg) * scale
        logits = torch.where(mask[None, None, :, :], logits, -1e9)
        attn = _softmax(logits, axis=-1)
        o = torch.einsum("bhsw,bhswd->bhsd", attn, vg)
        return (lambda x, *d: x.permute(*d))(o, 0, 2, 1, 3).reshape(B, S, D) @ wo

    def _B_ref(x, w1, b1, w2, b2):
        h = torch.einsum("bsd,df->bsf", x, w1) + b1
        h = _gelu(h)
        return torch.einsum("bsf,fd->bsd", h, w2) + b2

    x = A_x
    g = _sigmoid((x * gate_w).sum(-1, keepdims=True))
    a = _A_ref(x, A_wqkv, A_wo)
    b = _B_ref(x, B_w1, B_b1, B_w2, B_b2)
    return g * a + (1 - g) * b
```

```cpp
#include <hip/hip_runtime.h>
#include <hip/hip_bf16.h>
typedef __hip_bfloat16 bf16;
__device__ __forceinline__ float b2f(bf16 v){ return __bfloat162float(v); }
__device__ __forceinline__ bf16 f2b(float v){ return __float2bfloat16(v); }

__global__ void k_gemm(const bf16* A, const bf16* B, float* C, int M, int N, int K){
  __shared__ float As[16][17]; __shared__ float Bs[16][17];
  int tx=threadIdx.x&15, ty=threadIdx.x>>4;
  int row=blockIdx.y*16+ty, col=blockIdx.x*16+tx;
  float acc=0.f;
  for(int k0=0;k0<K;k0+=16){
    As[ty][tx]=b2f(A[(size_t)row*K+k0+tx]);
    Bs[ty][tx]=b2f(B[(size_t)(k0+ty)*N+col]);
    __syncthreads();
    for(int k=0;k<16;++k) acc+=As[ty][k]*Bs[k][tx];
    __syncthreads();
  }
  C[(size_t)row*N+col]=acc;
}
__global__ void k_attn(const float* qkv, bf16* o){
  int t=blockIdx.x*blockDim.x+threadIdx.x;
  int s=t>>5,h=t&31;
  float q[20];
  for(int d=0;d<20;++d) q[d]=b2f(f2b(qkv[(size_t)s*1920+h*20+d]));
  float lg[8]; float m=-3e38f;
  for(int w=0;w<8;++w){ int idx=s-2*w; bool ok=idx>=0; int ic=ok?idx:0; float a=0.f;
    for(int d=0;d<20;++d) a+=q[d]*b2f(f2b(qkv[(size_t)ic*1920+640+h*20+d]));
    a*=0.22360679774997896f; lg[w]=ok?a:-1e9f; m=fmaxf(m,lg[w]); }
  float sum=0.f; for(int w=0;w<8;++w){ lg[w]=__expf(lg[w]-m); sum+=lg[w]; }
  for(int d=0;d<20;++d){ float a=0.f; for(int w=0;w<8;++w){ int idx=s-2*w; int ic=idx>=0?idx:0; a+=lg[w]/sum*b2f(f2b(qkv[(size_t)ic*1920+1280+h*20+d])); }
    o[(size_t)s*640+h*20+d]=f2b(a); }
}
__global__ void k_gelu(const float* hp, const bf16* b1, bf16* h){
  int t=blockIdx.x*blockDim.x+threadIdx.x; int c=t%1536;
  float x=hp[t]+b2f(b1[c]);
  float u=0.7978845608028654f*(x+0.044715f*x*x*x);
  h[t]=f2b(0.5f*x*(1.f+tanhf(u)));
}
__global__ void k_final(const bf16* x, const bf16* gw, const float* a, const float* b, const bf16* b2, bf16* out){
  int s=blockIdx.x; __shared__ float red[256];
  float p=0.f; for(int i=threadIdx.x;i<640;i+=256) p+=b2f(x[(size_t)s*640+i])*b2f(gw[i]);
  red[threadIdx.x]=p; __syncthreads();
  for(int o=128;o>0;o>>=1){ if(threadIdx.x<o) red[threadIdx.x]+=red[threadIdx.x+o]; __syncthreads(); }
  float g=1.f/(1.f+__expf(-red[0]));
  for(int i=threadIdx.x;i<640;i+=256) out[(size_t)s*640+i]=f2b(g*a[(size_t)s*640+i]+(1.f-g)*(b[(size_t)s*640+i]+b2f(b2[i])));
}
extern "C" void kernel_launch(void* const* d_in, const int* in_sizes, int n_in, void* d_out, int out_size, void* d_ws, size_t ws_size, hipStream_t stream){
  const bf16* x=(const bf16*)d_in[0]; const bf16* wqkv=(const bf16*)d_in[1]; const bf16* wo=(const bf16*)d_in[2];
  const bf16* w1=(const bf16*)d_in[4]; const bf16* b1=(const bf16*)d_in[5]; const bf16* w2=(const bf16*)d_in[6]; const bf16* b2=(const bf16*)d_in[7]; const bf16* gw=(const bf16*)d_in[8];
  char* ws=(char*)d_ws;
  float* qkv=(float*)ws; ws+=(size_t)2048*1920*4;
  float* hp=(float*)ws; ws+=(size_t)2048*1536*4;
  float* a=(float*)ws; ws+=(size_t)2048*640*4;
  float* b=(float*)ws; ws+=(size_t)2048*640*4;
  bf16* o=(bf16*)ws; ws+=(size_t)2048*640*2;
  bf16* h=(bf16*)ws;
  k_gemm<<<dim3(1920/16,128),256,0,stream>>>(x,wqkv,qkv,2048,1920,640);
  k_gemm<<<dim3(1536/16,128),256,0,stream>>>(x,w1,hp,2048,1536,640);
  k_attn<<<2048*32/256,256,0,stream>>>(qkv,o);
  k_gelu<<<2048*1536/256,256,0,stream>>>(hp,b1,h);
  k_gemm<<<dim3(640/16,128),256,0,stream>>>(o,wo,a,2048,640,640);
  k_gemm<<<dim3(640/16,128),256,0,stream>>>(h,w2,b,2048,640,1536);
  k_final<<<2048,256,0,stream>>>(x,gw,a,b,b2,(bf16*)d_out);
}
```

```cpp
#include <hip/hip_runtime.h>
#include <hip/hip_bf16.h>
#include <stdint.h>

typedef __attribute__((ext_vector_type(8))) __bf16 bf16x8;
typedef __attribute__((ext_vector_type(4))) __bf16 bf16x4;
typedef __attribute__((ext_vector_type(2))) __bf16 bf16x2;
typedef __attribute__((ext_vector_type(4))) float f32x4;
typedef __attribute__((ext_vector_type(2))) float f32x2;
typedef __attribute__((ext_vector_type(4))) unsigned u32x4;
typedef __attribute__((ext_vector_type(2))) unsigned u32x2;
#define LAS __attribute__((address_space(3)))
#define DI __device__ __forceinline__

constexpr int S_ = 2048, D_ = 640, F_ = 1536;
constexpr int KS2 = 68;
constexpr unsigned Z_BYTES = 128u * KS2 * 1024u;

DI __amdgpu_buffer_rsrc_t make_srd(const void* p, unsigned bytes) {
  return __builtin_amdgcn_make_buffer_rsrc((void*)p, (short)0, (int)bytes, 0x00020000);
}
DI void dma16(unsigned voff, __amdgpu_buffer_rsrc_t srd, unsigned soff, unsigned lds) {
  asm volatile("s_mov_b32 m0, %0\n\ts_nop 0\n\tbuffer_load_dwordx4 %1, %2, %3 offen lds"
               :: "s"(lds), "v"(voff), "s"(srd), "s"(soff) : "memory");
}
DI bf16x8 lds_tr8(unsigned a0, unsigned a1) {
  bf16x4 lo = __builtin_amdgcn_ds_read_tr16_b64_v4bf16((LAS bf16x4*)a0);
  bf16x4 hi = __builtin_amdgcn_ds_read_tr16_b64_v4bf16((LAS bf16x4*)a1);
  return __builtin_shufflevector(lo, hi, 0, 1, 2, 3, 4, 5, 6, 7);
}
DI bf16x8 lds_b128(unsigned a) { return *(LAS bf16x8*)a; }
DI f32x4 mfma(bf16x8 a, bf16x8 b, f32x4 c) { return __builtin_amdgcn_mfma_f32_16x16x32_bf16(a, b, c, 0, 0, 0); }
DI unsigned pk2(float a, float b) { bf16x2 v = {(__bf16)a, (__bf16)b}; return __builtin_bit_cast(unsigned, v); }
DI u32x2 pk4(f32x4 v) { u32x2 r = {pk2(v[0], v[1]), pk2(v[2], v[3])}; return r; }
DI bf16x8 cat(u32x2 a, u32x2 b) { u32x4 r = {a[0], a[1], b[0], b[1]}; return __builtin_bit_cast(bf16x8, r); }
DI float bf2f(unsigned short h) { return __uint_as_float(((unsigned)h) << 16); }
DI float xg_max(float x) {
  auto s = __builtin_amdgcn_permlane16_swap(__float_as_uint(x), __float_as_uint(x), false, false);
  x = fmaxf(__uint_as_float(s[0]), __uint_as_float(s[1]));
  auto t = __builtin_amdgcn_permlane32_swap(__float_as_uint(x), __float_as_uint(x), false, false);
  return fmaxf(__uint_as_float(t[0]), __uint_as_float(t[1]));
}
DI float xg_sum(float x) {
  auto s = __builtin_amdgcn_permlane16_swap(__float_as_uint(x), __float_as_uint(x), false, false);
  x = __uint_as_float(s[0]) + __uint_as_float(s[1]);
  auto t = __builtin_amdgcn_permlane32_swap(__float_as_uint(x), __float_as_uint(x), false, false);
  return __uint_as_float(t[0]) + __uint_as_float(t[1]);
}
#define WAIT_BAR(N) asm volatile("s_waitcnt vmcnt(" #N ") lgkmcnt(0)\n\ts_barrier" ::: "memory")
#define LGKM_BAR() asm volatile("s_waitcnt lgkmcnt(0)\n\ts_barrier" ::: "memory")

DI unsigned swz23(unsigned k) { return (k & ~12u) | ((k & 4u) << 1) | ((k & 8u) >> 1); }

constexpr unsigned A_WB = 30720, A_STAGE = 49152;
constexpr unsigned M_WB = 24576, M_STAGE = 40960;
constexpr unsigned K1_LDS = 3 * A_STAGE;

struct K1Args { const void* x; const void* wqkv; const void* w1; const void* b1; const void* gw; void* z; float* gate; };

__global__ __launch_bounds__(512, 2) void k1(K1Args p) {
  extern __shared__ __attribute__((aligned(16))) char shm[];
  const unsigned lds0 = (unsigned)(uintptr_t)(LAS char*)shm;
  const int lane = threadIdx.x & 63;
  const int wid = __builtin_amdgcn_readfirstlane(threadIdx.x >> 6);
  const int b = blockIdx.x, xcd = b & 7, idx = b >> 3;
  const int rt = (xcd >> 1) * 4 + (idx & 3), c = idx >> 2, ch = xcd & 1;
  const bool is_attn = c < 4;
  const int G = 4 * ch + (c & 3);
  const int T0 = rt * 128;
  const int g = lane >> 4, q = (lane & 15) >> 2, pq = lane & 3, li = lane & 15;

  if (wid >= 4) {
    const int lw = wid - 4;
    auto sx = make_srd(p.x, S_ * D_ * 2);
    if (is_attn) {
      auto sw = make_srd(p.wqkv, D_ * 3 * D_ * 2);
      unsigned vo[12];
#pragma unroll
      for (int i = 0; i < 12; ++i) {
        int n = lw + 4 * i;
        unsigned cw = 64 * n + lane, pi = cw / 30, rem = cw % 30, sec = rem / 10, ci = rem % 10;
        unsigned wo_ = swz23(pi) * 3840u + sec * 1280u + 160u * G + 16u * ci;
        int r = 8 * (n - 30) + (lane >> 3);
        int tok = T0 - 16 + r; tok = tok < 0 ? 0 : tok;
        unsigned xo_ = (unsigned)tok * 1280u + 16u * ((lane & 7) ^ (lane >> 3));
        vo[i] = n < 30 ? wo_ : xo_;
      }
      const bool w7 = lw < 2;
      auto issue = [&](int st) {
        unsigned lb = lds0 + (unsigned)(st % 3) * A_STAGE + lw * 1024u;
        unsigned sow = st * 245760u, sox = st * 128u;
#pragma unroll
        for (int i = 0; i < 7; ++i) dma16(vo[i], sw, sow, lb + 4096u * i);
        if (w7) dma16(vo[7], sw, sow, lb + 4096u * 7); else dma16(vo[7], sx, sox, lb + 4096u * 7);
#pragma unroll
        for (int i = 8; i < 12; ++i) dma16(vo[i], sx, sox, lb + 4096u * i);
      };
      issue(0); issue(1);
      for (int st = 0; st < 8; ++st) { WAIT_BAR(12); issue(st + 2); }
      WAIT_BAR(12);
      WAIT_BAR(0);
    } else {
      auto sw = make_srd(p.w1, D_ * F_ * 2);
      unsigned vo[10];
#pragma unroll
      for (int i = 0; i < 10; ++i) {
        int n = lw + 4 * i;
        unsigned cw = 64 * n + lane, pi = cw / 24, cp = cw % 24;
        unsigned sp = (cp >> 1) ^ ((pi >> 1) & 3);
        unsigned wo_ = swz23(pi) * 3072u + 384u * G + 32u * sp + 16u * (cp & 1);
        int r = 8 * (n - 24) + (lane >> 3);
        unsigned xo_ = (unsigned)(T0 + r) * 1280u + 16u * ((lane & 7) ^ (lane >> 3));
        vo[i] = n < 24 ? wo_ : xo_;
      }
      auto issue = [&](int st) {
        unsigned lb = lds0 + (unsigned)(st % 3) * M_STAGE + lw * 1024u;
        unsigned sow = st * 196608u, sox = st * 128u;
#pragma unroll
        for (int i = 0; i < 6; ++i) dma16(vo[i], sw, sow, lb + 4096u * i);
#pragma unroll
        for (int i = 6; i < 10; ++i) dma16(vo[i], sx, sox, lb + 4096u * i);
      };
      issue(0); issue(1);
      for (int st = 0; st < 8; ++st) { WAIT_BAR(10); issue(st + 2); }
      WAIT_BAR(10);
      WAIT_BAR(0);
    }
    {
      const int cc = is_attn ? G : 8 + G;
      const int row = T0 + 8 * cc + 2 * lw + (lane >> 5);
      const int l5 = lane & 31;
      const u32x4* xr = (const u32x4*)((const char*)p.x + (size_t)row * 1280);
      const u32x4* gr = (const u32x4*)p.gw;
      float acc = 0.f;
#pragma unroll
      for (int j = 0; j < 3; ++j) {
        int ck = l5 + 32 * j;
        bool ok = ck < 80;
        int ckc = ok ? ck : 0;
        u32x4 xv = xr[ckc], gv = gr[ckc];
        float s = 0.f;
#pragma unroll
        for (int e = 0; e < 4; ++e) {
          s += __uint_as_float(xv[e] << 16) * __uint_as_float(gv[e] << 16);
          s += __uint_as_float(xv[e] & 0xffff0000u) * __uint_as_float(gv[e] & 0xffff0000u);
        }
        acc += ok ? s : 0.f;
      }
#pragma unroll
      for (int o = 16; o > 0; o >>= 1) acc += __shfl_xor(acc, o);
      if (l5 == 0) p.gate[row] = 1.f / (1.f + __expf(-acc));
    }
    return;
  }

  const int w = wid;
  const unsigned lrow = 16 * (g >> 1) + 4 * (g & 1) + q;
  if (is_attn) {
    unsigned wa[5];
#pragma unroll
    for (int i = 0; i < 4; ++i) wa[i] = lds0 + lrow * 480u + 40u * ((i + w) & 3) + 8u * pq;
    wa[4] = lds0 + lrow * 480u + 40u * pq + 32u;
    unsigned xa[2], xh[2];
#pragma unroll
    for (int ks = 0; ks < 2; ++ks) {
      unsigned sw = 16u * ((4 * ks + g) ^ (lane & 7));
      xh[ks] = lds0 + A_WB + li * 128u + sw;
      xa[ks] = xh[ks] + 2048u * (1 + 2 * w);
    }
    f32x4 acc[15][2], hk0 = {}, hkx = {}, hv0 = {}, hvx = {};
#pragma unroll
    for (int u = 0; u < 15; ++u) { acc[u][0] = f32x4{}; acc[u][1] = f32x4{}; }

    for (int st = 0; st < 10; ++st) {
      LGKM_BAR();
      const unsigned sb = (unsigned)(st % 3) * A_STAGE;
#pragma unroll
      for (int ks = 0; ks < 2; ++ks) {
        bf16x8 x0 = lds_b128(xa[ks] + sb), x1 = lds_b128(xa[ks] + sb + 2048u), xhf = lds_b128(xh[ks] + sb);
#pragma unroll
        for (int u = 0; u < 15; ++u) {
          const int sec = u / 5, i = u % 5;
          const unsigned imm = (32 * ks) * 480u + sec * 160u;
          bf16x8 wf = lds_tr8(wa[i] + sb + imm, wa[i] + sb + imm + 8 * 480u);
          if (sec < 2) {
            acc[u][0] = mfma(wf, x0, acc[u][0]);
            acc[u][1] = mfma(wf, x1, acc[u][1]);
            if (u == 5) hk0 = mfma(wf, xhf, hk0);
            if (u == 9) hkx = mfma(wf, xhf, hkx);
          } else {
            acc[u][0] = mfma(x0, wf, acc[u][0]);
            acc[u][1] = mfma(x1, wf, acc[u][1]);
            if (u == 10) hv0 = mfma(xhf, wf, hv0);
            if (u == 14) hvx = mfma(xhf, wf, hvx);
          }
        }
      }
    }
    u32x2 Qp[5][2], Kp[5][2], Vp[5][2];
#pragma unroll
    for (int i = 0; i < 5; ++i)
#pragma unroll
      for (int bk = 0; bk < 2; ++bk) { Qp[i][bk] = pk4(acc[i][bk]); Kp[i][bk] = pk4(acc[5 + i][bk]); Vp[i][bk] = pk4(acc[10 + i][bk]); }
    LGKM_BAR();
    {
      const unsigned eb = lds0 + lane * 16u;
      if (w < 3) {
#pragma unroll
        for (int i = 0; i < 5; ++i) {
          unsigned slot = i < 4 ? ((i + w) & 3) : 4;
          u32x4 v = {Kp[i][1][0], Kp[i][1][1], Vp[i][1][0], Vp[i][1][1]};
          *(LAS u32x4*)(eb + (w + 1) * 5120u + slot * 1024u) = v;
        }
      }
      u32x2 a = pk4(hk0), bb = pk4(hv0);
      u32x4 v = {a[0], a[1], bb[0], bb[1]};
      *(LAS u32x4*)(eb + w * 1024u) = v;
      if (w == 0) {
        u32x2 a2 = pk4(hkx), b2 = pk4(hvx);
        u32x4 v2 = {a2[0], a2[1], b2[0], b2[1]};
        *(LAS u32x4*)(eb + 4 * 1024u) = v2;
      }
    }
    LGKM_BAR();
    u32x2 Kh[5], Vh[5];
    {
      const unsigned eb = lds0 + lane * 16u + w * 5120u;
#pragma unroll
      for (int i = 0; i < 5; ++i) {
        unsigned slot = i < 4 ? ((i + w) & 3) : 4;
        u32x4 v = *(LAS u32x4*)(eb + slot * 1024u);
        Kh[i] = u32x2{v[0], v[1]}; Vh[i] = u32x2{v[2], v[3]};
      }
    }
    const int par = li & 1;
    const bool cura = (4 * g + par) <= li, curb = (4 * g + par + 2) <= li;
    const unsigned hm = par ? 0xffff0000u : 0x0000ffffu;
    const unsigned ma0 = cura ? 0u : hm, ma1 = cura ? hm : 0u, mb0 = curb ? 0u : hm, mb1 = curb ? hm : 0u;
    const float SC = 0.22360679774997896f * 1.4426950408889634f, NEG = -1e9f * 1.4426950408889634f;
    const bool first = (rt == 0) && (w == 0);
    char* zb = (char*)p.z;
#pragma unroll
    for (int bk = 0; bk < 2; ++bk) {
      const bool fm = first && bk == 0;
      const float sca = (fm && !cura) ? 0.f : SC, bia = (fm && !cura) ? NEG : 0.f;
      const float scb = (fm && !curb) ? 0.f : SC, bib = (fm && !curb) ? NEG : 0.f;
      f32x4 ox = {};
      const int tb = rt * 8 + 2 * w + bk;
#pragma unroll
      for (int i = 0; i < 4; ++i) {
        const int a = (i + w) & 3;
        const unsigned mq = (g == a) ? 0xffffffffu : 0u, mv = (q == a) ? 0xffffffffu : 0u;
        u32x2 qx = {Qp[4][bk][0] & mq, Qp[4][bk][1] & mq};
        bf16x8 Qf = cat(Qp[i][bk], qx);
        bf16x8 Kprev = bk == 0 ? cat(Kh[i], Kh[4]) : cat(Kp[i][0], Kp[4][0]);
        bf16x8 Kcur = cat(Kp[i][bk], Kp[4][bk]);
        f32x4 s0 = mfma(Kprev, Qf, f32x4{}), s1 = mfma(Kcur, Qf, f32x4{});
        float s0a = par ? s0[1] : s0[0], s0b = par ? s0[3] : s0[2];
        float s1a = par ? s1[1] : s1[0], s1b = par ? s1[3] : s1[2];
        float la = (cura ? s1a : s0a) * sca + bia, lb = (curb ? s1b : s0b) * scb + bib;
        float m = xg_max(fmaxf(la, lb));
        float ea = __builtin_amdgcn_exp2f(la - m), eb = __builtin_amdgcn_exp2f(lb - m);
        float inv = __builtin_amdgcn_rcpf(xg_sum(ea + eb));
        unsigned ta = pk2(ea * inv, ea * inv), tbb = pk2(eb * inv, eb * inv);
        u32x4 pv = {ta & ma0, tbb & mb0, ta & ma1, tbb & mb1};
        bf16x8 P = __builtin_bit_cast(bf16x8, pv);
        u32x2 vprev = bk == 0 ? Vh[i] : Vp[i][0], vxp = bk == 0 ? Vh[4] : Vp[4][0];
        f32x4 om = mfma(cat(vprev, Vp[i][bk]), P, f32x4{});
        u32x2 vx0 = {vxp[0] & mv, vxp[1] & mv}, vx1 = {Vp[4][bk][0] & mv, Vp[4][bk][1] & mv};
        ox = mfma(cat(vx0, vx1), P, ox);
        *(u32x2*)(zb + ((size_t)(tb * KS2 + 2 * G + (a >> 1)) * 64 + lane) * 16 + 8 * (a & 1)) = pk4(om);
      }
      *(u32x2*)(zb + ((size_t)(tb * KS2 + 16 + (G >> 1)) * 64 + lane) * 16 + 8 * (G & 1)) = pk4(ox);
    }
  } else {
    const int wr = w >> 1, wc = w & 1;
    const unsigned f = 2 * (g & 1) + (q >> 1);
    unsigned wa[6];
#pragma unroll
    for (int n = 0; n < 6; ++n) wa[n] = lds0 + lrow * 384u + 32u * ((6 * wc + n) ^ f) + 8u * pq;
    unsigned xa[2];
#pragma unroll
    for (int ks = 0; ks < 2; ++ks) xa[ks] = lds0 + M_WB + (64 * wr + li) * 128u + 16u * ((4 * ks + g) ^ (lane & 7));
    f32x4 acc[6][4];
#pragma unroll
    for (int n = 0; n < 6; ++n) {
      u32x2 bv = *(const u32x2*)((const char*)p.b1 + (192 * G + 96 * wc + 16 * n + 4 * g) * 2);
      f32x4 bi = {__uint_as_float(bv[0] << 16), __uint_as_float(bv[0] & 0xffff0000u), __uint_as_float(bv[1] << 16), __uint_as_float(bv[1] & 0xffff0000u)};
#pragma unroll
      for (int m = 0; m < 4; ++m) acc[n][m] = bi;
    }
    for (int st = 0; st < 10; ++st) {
      LGKM_BAR();
      const unsigned sb = (unsigned)(st % 3) * M_STAGE;
#pragma unroll
      for (int ks = 0; ks < 2; ++ks) {
        bf16x8 xf[4];
#pragma unroll
        for (int m = 0; m < 4; ++m) xf[m] = lds_b128(xa[ks] + sb + 2048u * m);
#pragma unroll
        for (int n = 0; n < 6; ++n) {
          const unsigned imm = (32 * ks) * 384u;
          bf16x8 wf = lds_tr8(wa[n] + sb + imm, wa[n] + sb + imm + 8 * 384u);
#pragma unroll
          for (int m = 0; m < 4; ++m) acc[n][m] = mfma(wf, xf[m], acc[n][m]);
        }
      }
    }
    const float C1 = -2.f * 0.7978845608028654f * 1.4426950408889634f, C2 = C1 * 0.044715f;
    char* zb = (char*)p.z;
#pragma unroll
    for (int m = 0; m < 4; ++m) {
      const int tb = rt * 8 + 4 * wr + m;
#pragma unroll
      for (int j = 0; j < 3; ++j) {
        u32x4 o;
#pragma unroll
        for (int e = 0; e < 2; ++e) {
          f32x4 v = acc[2 * j + e][m], r;
#pragma unroll
          for (int k = 0; k < 4; ++k) {
            float x = v[k];
            float ex = __builtin_amdgcn_exp2f(x * (C1 + C2 * x * x));
            r[k] = x * __builtin_amdgcn_rcpf(1.f + ex);
          }
          u32x2 pp = pk4(r);
          o[2 * e] = pp[0]; o[2 * e + 1] = pp[1];
        }
        *(u32x4*)(zb + ((size_t)(tb * KS2 + 20 + 6 * G + 3 * wc + j) * 64 + lane) * 16) = o;
      }
    }
  }
}

constexpr unsigned K2_STAGE = 20480, K2_NB = 7, K2_LDS = K2_STAGE * K2_NB;
struct K2Args { const void* z; const void* wo; const void* w2; const void* b2; const float* gate; void* out; };

__global__ __launch_bounds__(512, 2) void k2(K2Args p) {
  extern __shared__ __attribute__((aligned(16))) char shm[];
  const unsigned lds0 = (unsigned)(uintptr_t)(LAS char*)shm;
  const int lane = threadIdx.x & 63;
  const int wid = __builtin_amdgcn_readfirstlane(threadIdx.x >> 6);
  const int b = blockIdx.x, xcd = b & 7, idx = b >> 3;
  const int rq = xcd >> 1, ch = xcd & 1;
  const int rti = idx % 6, cn = 5 * ch + idx / 6;
  const int tb0 = rq * 32 + rti * 6;
  const int nblk = rti < 5 ? 6 : 2;
  const int g = lane >> 4, q = (lane & 15) >> 2, pq = lane & 3, li = lane & 15;

  if (wid >= 4) {
    const int lw = wid - 4;
    auto sz = make_srd(p.z, Z_BYTES);
    auto so = make_srd(p.wo, D_ * D_ * 2);
    auto s2 = make_srd(p.w2, F_ * D_ * 2);
    const unsigned rho = 8 * lw + (lane >> 3), t = rho >> 4, gg = (rho >> 2) & 3, qq = rho & 3;
    const unsigned cpos = lane & 7, sch = (((cpos >> 1) ^ ((rho >> 1) & 3)) << 1) | (cpos & 1);
    const unsigned cb = 128u * cn + 16u * sch;
    const unsigned vA = (20 * t + (rho & 15)) * 1280u + cb;
    const unsigned vB = (80 * t + 20 * gg + 16 + qq) * 1280u + cb;
    const unsigned vC = rho * 1280u + cb;
    const unsigned vz = lane * 16u;
    auto issue = [&](int st) {
      unsigned lb = lds0 + (unsigned)(st % K2_NB) * K2_STAGE;
#pragma unroll
      for (int i = 0; i < 3; ++i) {
        int n = 3 * lw + i;
        int blk = n >> 1; blk = blk < nblk ? blk : 0;
        dma16(vz, sz, (unsigned)(((tb0 + blk) * KS2 + 2 * st + (n & 1)) * 1024), lb + 1024u * n);
      }
#pragma unroll
      for (int e = 0; e < 2; ++e) {
        unsigned l2 = lb + 12288u + 4096u * e + 1024u * lw;
        int ks = 2 * st + e;
        if (st < 8) dma16(vA, so, (unsigned)(40 * ks * 1280), l2);
        else if (st < 10) dma16(vB, so, (unsigned)(160 * (ks - 16) * 1280), l2);
        else dma16(vC, s2, (unsigned)(32 * (ks - 20) * 1280), l2);
      }
    };
    for (int st = 0; st < 6; ++st) issue(st);
    for (int st = 0; st < 28; ++st) { WAIT_BAR(25); issue(st + 6); }
    WAIT_BAR(25); WAIT_BAR(20); WAIT_BAR(15); WAIT_BAR(10); WAIT_BAR(5); WAIT_BAR(0);
    return;
  }
  const int w = wid, wr = w >> 1, wc = w & 1;
  const unsigned za = lds0 + wr * 3 * 2048u + lane * 16u;
  unsigned wa[2];
  {
    const unsigned r = 4 * g + q;
#pragma unroll
    for (int n = 0; n < 2; ++n) wa[n] = lds0 + 12288u + r * 128u + 32u * ((2 * wc + n) ^ ((r >> 1) & 3)) + 8u * pq;
  }
  f32x4 acc[2][2][3];
#pragma unroll
  for (int h = 0; h < 2; ++h)
#pragma unroll
    for (int n = 0; n < 2; ++n)
#pragma unroll
      for (int m = 0; m < 3; ++m) acc[h][n][m] = f32x4{};
  auto stage = [&](int st, f32x4 (&a)[2][3]) {
    LGKM_BAR();
    const unsigned sb = (unsigned)(st % K2_NB) * K2_STAGE;
#pragma unroll
    for (int e = 0; e < 2; ++e) {
      bf16x8 zf[3], wf[2];
#pragma unroll
      for (int m = 0; m < 3; ++m) zf[m] = lds_b128(za + sb + 2048u * m + 1024u * e);
#pragma unroll
      for (int n = 0; n < 2; ++n) wf[n] = lds_tr8(wa[n] + sb + 4096u * e, wa[n] + sb + 4096u * e + 2048u);
#pragma unroll
      for (int n = 0; n < 2; ++n)
#pragma unroll
        for (int m = 0; m < 3; ++m) a[n][m] = mfma(wf[n], zf[m], a[n][m]);
    }
  };
  for (int st = 0; st < 10; ++st) stage(st, acc[0]);
  for (int st = 10; st < 34; ++st) stage(st, acc[1]);
#pragma unroll
  for (int m = 0; m < 3; ++m) {
    const int blk = 3 * wr + m;
    if (blk < nblk) {
      const int tok = (tb0 + blk) * 16 + li;
      const float gt = p.gate[tok];
#pragma unroll
      for (int n = 0; n < 2; ++n) {
        const int col = 64 * cn + 32 * wc + 16 * n + 4 * g;
        u32x2 bv = *(const u32x2*)((const char*)p.b2 + col * 2);
        f32x4 bi = {__uint_as_float(bv[0] << 16), __uint_as_float(bv[0] & 0xffff0000u), __uint_as_float(bv[1] << 16), __uint_as_float(bv[1] & 0xffff0000u)};
        f32x4 r = acc[0][n][m] * gt + (acc[1][n][m] + bi) * (1.f - gt);
        *(u32x2*)((char*)p.out + ((size_t)tok * D_ + col) * 2) = pk4(r);
      }
    }
  }
}

extern "C" void kernel_launch(void* const* d_in, const int* in_sizes, int n_in, void* d_out, int out_size, void* d_ws,
                              size_t ws_size, hipStream_t stream) {
  (void)in_sizes; (void)n_in; (void)out_size; (void)ws_size;
  hipFuncSetAttribute((const void*)k1, hipFuncAttributeMaxDynamicSharedMemorySize, K1_LDS);
  hipFuncSetAttribute((const void*)k2, hipFuncAttributeMaxDynamicSharedMemorySize, K2_LDS);
  char* ws = (char*)d_ws;
  K1Args a1{d_in[0], d_in[1], d_in[4], d_in[5], d_in[8], ws, (float*)(ws + Z_BYTES)};
  K2Args a2{ws, d_in[2], d_in[6], d_in[7], (const float*)(ws + Z_BYTES), d_out};
  k1<<<256, 512, K1_LDS, stream>>>(a1);
  k2<<<240, 512, K2_LDS, stream>>>(a2);
}
```

```cpp
#include <hip/hip_runtime.h>
#include <hip/hip_bf16.h>
#include <stdint.h>

typedef __attribute__((ext_vector_type(8))) __bf16 bf16x8;
typedef __attribute__((ext_vector_type(4))) __bf16 bf16x4;
typedef __attribute__((ext_vector_type(2))) __bf16 bf16x2;
typedef __attribute__((ext_vector_type(4))) float f32x4;
typedef __attribute__((ext_vector_type(2))) float f32x2;
typedef __attribute__((ext_vector_type(4))) unsigned u32x4;
typedef __attribute__((ext_vector_type(2))) unsigned u32x2;
typedef __attribute__((ext_vector_type(8))) unsigned u32x8;
typedef __amdgpu_buffer_rsrc_t srd_t;
#define LAS __attribute__((address_space(3)))
#define DI __device__ __forceinline__

constexpr int S_ = 2048, D_ = 640, F_ = 1536;
constexpr int KS2 = 68;
constexpr unsigned Z_BYTES = 128u * KS2 * 1024u;

DI srd_t make_srd(const void* p, unsigned bytes) {
  return __builtin_amdgcn_make_buffer_rsrc((void*)p, (short)0, (int)bytes, 0x00020000);
}
DI void dma16(unsigned voff, srd_t srd, unsigned soff, unsigned lds) {
  asm volatile("s_mov_b32 m0, %0\n\ts_nop 0\n\tbuffer_load_dwordx4 %1, %2, %3 offen lds"
               :: "s"(lds), "v"(voff), "s"(srd), "s"(soff) : "memory");
}
__device__ const unsigned g_seq[64] = {0,  1,  2,  3,  4,  5,  6,  7,  8,  9,  10, 11, 12, 13, 14, 15, 16, 17, 18, 19, 20, 21,
                                       22, 23, 24, 25, 26, 27, 28, 29, 30, 31, 32, 33, 34, 35, 36, 37, 38, 39, 40, 41, 42, 43,
                                       44, 45, 46, 47, 48, 49, 50, 51, 52, 53, 54, 55, 56, 57, 58, 59, 60, 61, 62, 63};
DI void dma4(unsigned voff, srd_t srd, unsigned soff, unsigned lds) {
  asm volatile("s_mov_b32 m0, %0\n\ts_nop 0\n\tbuffer_load_dword %1, %2, %3 offen lds"
               :: "s"(lds), "v"(voff), "s"(srd), "s"(soff) : "memory");
}
DI void touch(const char* addr, unsigned lds_scratch) {
  asm volatile("s_mov_b32 m0, %0\n\ts_nop 0\n\tglobal_load_lds_dword %1, off" :: "s"(lds_scratch), "v"(addr) : "memory");
}
template <unsigned HI> DI void dma16m(unsigned voff, srd_t srd, unsigned soff, unsigned lds) {
  asm volatile("s_mov_b32 m0, %0\n\ts_mov_b32 exec_hi, %4\n\tbuffer_load_dwordx4 %1, %2, %3 offen lds\n\ts_mov_b32 exec_hi, -1"
               :: "s"(lds), "v"(voff), "s"(srd), "s"(soff), "i"(HI) : "memory");
}
DI bf16x8 lds_tr8(unsigned a0, unsigned a1) {
  bf16x4 lo = __builtin_amdgcn_ds_read_tr16_b64_v4bf16((LAS bf16x4*)(size_t)a0);
  bf16x4 hi = __builtin_amdgcn_ds_read_tr16_b64_v4bf16((LAS bf16x4*)(size_t)a1);
  return __builtin_shufflevector(lo, hi, 0, 1, 2, 3, 4, 5, 6, 7);
}
DI bf16x4 lds_tr4(unsigned a) { return __builtin_amdgcn_ds_read_tr16_b64_v4bf16((LAS bf16x4*)(size_t)a); }
DI bf16x8 cat8(bf16x4 lo, bf16x4 hi) { return __builtin_shufflevector(lo, hi, 0, 1, 2, 3, 4, 5, 6, 7); }
DI bf16x8 lds_b128(unsigned a) { return *(LAS bf16x8*)(size_t)a; }
DI f32x4 mfma(bf16x8 a, bf16x8 b, f32x4 c) { return __builtin_amdgcn_mfma_f32_16x16x32_bf16(a, b, c, 0, 0, 0); }
DI f32x4 mfmau(u32x4 a, u32x4 b, f32x4 c) { return mfma(__builtin_bit_cast(bf16x8, a), __builtin_bit_cast(bf16x8, b), c); }
DI unsigned pk2(float a, float b) { bf16x2 v = {(__bf16)a, (__bf16)b}; return __builtin_bit_cast(unsigned, v); }
DI u32x2 pk4(f32x4 v) { u32x2 r = {pk2(v[0], v[1]), pk2(v[2], v[3])}; return r; }
DI float vmax(float a, float b) { return __builtin_amdgcn_fmed3f(a, b, __builtin_inff()); }
DI float xg_max(float x) {
  auto s = __builtin_amdgcn_permlane16_swap(__float_as_uint(x), __float_as_uint(x), false, false);
  x = vmax(__uint_as_float(s[0]), __uint_as_float(s[1]));
  auto t = __builtin_amdgcn_permlane32_swap(__float_as_uint(x), __float_as_uint(x), false, false);
  return vmax(__uint_as_float(t[0]), __uint_as_float(t[1]));
}
DI float xg_sum(float x) {
  auto s = __builtin_amdgcn_permlane16_swap(__float_as_uint(x), __float_as_uint(x), false, false);
  x = __uint_as_float(s[0]) + __uint_as_float(s[1]);
  auto t = __builtin_amdgcn_permlane32_swap(__float_as_uint(x), __float_as_uint(x), false, false);
  return __uint_as_float(t[0]) + __uint_as_float(t[1]);
}
DI f32x4 bf4_to_f32(u32x2 v) {
  return f32x4{__uint_as_float(v[0] << 16), __uint_as_float(v[0] & 0xffff0000u), __uint_as_float(v[1] << 16), __uint_as_float(v[1] & 0xffff0000u)};
}
#define STAMP_DECL
#define STAMP(slot)
#define STAMPL(slot)
#define STAMP_RT(slot)
#define STAMP_OUT(kid)
#define SST(x) (x)
#define ISS(x) x
#define MF(c, a, b) asm volatile("v_mfma_f32_16x16x32_bf16 %0, %1, %2, %0" : "+v"(c) : "v"(a), "v"(b))
#define SB() __builtin_amdgcn_sched_barrier(0)
#define OPAQUE(x) asm volatile("" : "+v"(x))
#define WAITV(N) asm volatile("s_waitcnt vmcnt(" #N ")" ::: "memory")
#define LGKM_BAR() asm volatile("s_waitcnt lgkmcnt(0)\n\ts_barrier" ::: "memory")
#define FENCE() asm volatile("" ::: "memory")

DI void prog_set(unsigned row_addr  , unsigned v) {
  FENCE();
  __hip_atomic_store((LAS unsigned*)(size_t)row_addr, v, __ATOMIC_RELAXED, __HIP_MEMORY_SCOPE_WORKGROUP);
  FENCE();
}
DI unsigned prog_read(unsigned addr  ) { return __hip_atomic_load((LAS unsigned*)(size_t)addr, __ATOMIC_RELAXED, __HIP_MEMORY_SCOPE_WORKGROUP); }
DI void prog_wait(unsigned addr, unsigned seen, unsigned need) {
  if (__builtin_expect(__builtin_amdgcn_ballot_w64(seen < need) != 0, 0)) {
    unsigned long long m;
    do {
      unsigned v;
      asm volatile("ds_read_b32 %0, %1\n\ts_waitcnt lgkmcnt(0)" : "=v"(v) : "v"(addr) : "memory");
      m = __builtin_amdgcn_ballot_w64(v < need);
    } while (m != 0);
  }
  FENCE();
}
DI void prog_wait_sleep(unsigned addr, unsigned need) {
  while (__builtin_amdgcn_ballot_w64(prog_read(addr) < need) != 0) __builtin_amdgcn_s_sleep(1);
  FENCE();
}

DI unsigned swz23(unsigned k) { return (k & ~12u) | ((k & 4u) << 1) | ((k & 8u) >> 1); }

constexpr unsigned A_WB = 30720, A_STAGE = 49152;
constexpr unsigned M_WB = 24576, M_STAGE = 40960;
constexpr unsigned M_BIAS = 3 * M_STAGE;
constexpr unsigned K1_FLAGS = 3 * A_STAGE, K1_LDS = K1_FLAGS + 2048 + 256;

struct K1Args { const void* x; const void* wqkv; const void* w1; const void* b1; const void* gw; void* z; float* gate; const void* wo; const void* w2; const void* out; const void* b2; };

__global__ __launch_bounds__(512, 2) void k1(K1Args p) {
  extern __shared__ __attribute__((aligned(16))) char shm[];
  const unsigned lds0 = (unsigned)(uintptr_t)(LAS char*)shm;
  const int lane = threadIdx.x & 63;
  const int wid = __builtin_amdgcn_readfirstlane(threadIdx.x >> 6);
  const int b = blockIdx.x, xcd = b & 7, idx = b >> 3;
  const int rt = (xcd >> 1) * 4 + (idx & 3), c = idx >> 2, ch = xcd & 1;
  const bool is_attn = c < 4;
  const int G = 4 * ch + (c & 3);
  const int T0 = rt * 128;
  const unsigned f_own = lds0 + K1_FLAGS + wid * 256u + lane * 4u;
  const unsigned f_oth = lds0 + K1_FLAGS + (wid < 4 ? 1024u : 0u) + (lane & 3) * 260u;
  prog_set(f_own, 0u);
  STAMP_DECL
  STAMP(0); STAMP_RT(61);

  if (wid < 4) {
    const int lw = wid;
    const srd_t sx = make_srd(p.x, S_ * D_ * 2);
    const srd_t sq = make_srd(g_seq, 256);
    const unsigned vzero = 0, f_row = lds0 + K1_FLAGS + wid * 256u;
    const unsigned vx = (lane >> 3) * 1280u + 16u * ((lane & 7) ^ (lane >> 3));
    if (is_attn) {
      const srd_t sw = make_srd(p.wqkv, D_ * 3 * D_ * 2);
      const unsigned r2 = lane >= 30 ? 1u : 0u, rem = lane - 30u * r2, sec = (rem >= 20 ? 2u : (rem >= 10 ? 1u : 0u));
      const unsigned vw = r2 * 3840u + sec * 1280u + 16u * (rem - 10u * sec);
      const int hr = 4 * lw + (lane >> 3);
      const int ht = T0 - 16 + hr;
      const unsigned vh = (unsigned)(ht < 0 ? 0 : ht) * 1280u + 16u * ((lane & 7) ^ (hr & 7));
      auto issueA = [&](int st, unsigned lb) {
        const unsigned sow = st * 245760u + 160u * G, sox = st * 128u + (unsigned)T0 * 1280u;
        dma16m<0u>(vh, sx, st * 128u, lb + A_WB + 512u * lw);
#pragma unroll
        for (int i = 0; i < 4; ++i) { const unsigned n = 4 * lw + i; dma16(vx, sx, sox + n * 10240u, lb + A_WB + 2048u + 1024u * n); }
#pragma unroll
        for (int i = 0; i < 2; ++i) { const unsigned n = 8 * lw + i; dma16m<0x0fffffffu>(vw, sw, sow + swz23(2 * n) * 3840u, lb + 960u * n); }
      };
      auto issueB = [&](int st, unsigned lb) {
        const unsigned sow = st * 245760u + 160u * G;
#pragma unroll
        for (int i = 2; i < 8; ++i) { const unsigned n = 8 * lw + i; dma16m<0x0fffffffu>(vw, sw, sow + swz23(2 * n) * 3840u, lb + 960u * n); }
      };
      STAMP(1);
      issueA(0, lds0); issueB(0, lds0);
      LGKM_BAR();
      dma4(vzero, sq, 4, f_row);
      issueA(1, lds0 + A_STAGE); issueB(1, lds0 + A_STAGE); dma4(vzero, sq, 8, f_row);
      issueA(2, lds0 + 2 * A_STAGE); issueB(2, lds0 + 2 * A_STAGE); dma4(vzero, sq, 12, f_row);
      unsigned lb = lds0;
      for (int st = 3; st < 10; ++st) {
        prog_wait_sleep(f_oth, st - 2);
        ISS(issueA(SST(st), lb));
        ISS(issueB(SST(st), lb));
        dma4(vzero, sq, 4 * st + 4, f_row);
        STAMPL(1 + st);
        lb = lb == lds0 + 2 * A_STAGE ? lds0 : lb + A_STAGE;
      }
    } else {
      const srd_t sw = make_srd(p.w1, D_ * F_ * 2);
      unsigned vw[3];
#pragma unroll
      for (int i = 0; i < 3; ++i) {
        const unsigned cw = 64 * i + lane, j = (cw * 171u) >> 12, cp = cw - 24u * j;
        vw[i] = (8u * (j >> 2) + (j & 3)) * 3072u + 32u * ((cp >> 1) ^ ((j >> 1) & 3)) + 16u * (cp & 1);
      }
      auto issueA = [&](int st, unsigned lb) {
        const unsigned sow = st * 196608u + 384u * G, sox = st * 128u + (unsigned)T0 * 1280u;
#pragma unroll
        for (int i = 0; i < 4; ++i) { const unsigned n = 4 * lw + i; dma16(vx, sx, sox + n * 10240u, lb + M_WB + 1024u * n); }
#pragma unroll
        for (int i = 0; i < 3; ++i) dma16(vw[i], sw, sow + (16u * (lw >> 1) + 4u * (lw & 1)) * 3072u, lb + 3072u * lw + 1024u * i);
      };
      auto issueB = [&](int st, unsigned lb) {
        const unsigned sow = st * 196608u + 384u * G;
#pragma unroll
        for (int i = 0; i < 3; ++i) dma16(vw[i], sw, sow + (32u + 16u * (lw >> 1) + 4u * (lw & 1)) * 3072u, lb + 3072u * (lw + 4) + 1024u * i);
      };
      STAMP(1);
      issueA(0, lds0); issueB(0, lds0);
      if (lw == 0) dma16(lane * 16u, make_srd(p.b1, F_ * 2), 384u * G, lds0 + M_BIAS);
      LGKM_BAR();
      dma4(vzero, sq, 4, f_row);
      issueA(1, lds0 + M_STAGE); issueB(1, lds0 + M_STAGE); dma4(vzero, sq, 8, f_row);
      issueA(2, lds0 + 2 * M_STAGE); issueB(2, lds0 + 2 * M_STAGE); dma4(vzero, sq, 12, f_row);
      unsigned lb = lds0;
      for (int st = 3; st < 10; ++st) {
        prog_wait_sleep(f_oth, st - 2);
        ISS(issueA(SST(st), lb));
        ISS(issueB(SST(st), lb));
        dma4(vzero, sq, 4 * st + 4, f_row);
        STAMPL(1 + st);
        lb = lb == lds0 + 2 * M_STAGE ? lds0 : lb + M_STAGE;
      }
#pragma unroll
      for (int j = 0; j < 3; ++j) {
        int L = (j * 64 + (idx - 16) * 4 + lw) * 64 + lane;
        L = L < 10880 ? L : 0;
        const int r = L / 5, cl = L % 5;
        const char* pa = (r < 640 ? (const char*)p.wo + r * 1280 : (const char*)p.w2 + (r - 640) * 1280) + ch * 640 + cl * 128;
        touch(pa, lds0 + K1_FLAGS + 2048u);
      }
    }
    STAMP(20);
    {
      const int cc = is_attn ? G : 8 + G;
      const int row = T0 + 8 * cc + 2 * lw + (lane >> 5);
      const int l5 = lane & 31;
      const u32x4* xr = (const u32x4*)((const char*)p.x + (size_t)row * 1280);
      const u32x4* gr = (const u32x4*)p.gw;
      float acc = 0.f;
#pragma unroll
      for (int j = 0; j < 3; ++j) {
        int ck = l5 + 32 * j;
        bool ok = ck < 80;
        int ckc = ok ? ck : 0;
        u32x4 xv = xr[ckc], gv = gr[ckc];
        float s = 0.f;
#pragma unroll
        for (int e = 0; e < 4; ++e) {
          s += __uint_as_float(xv[e] << 16) * __uint_as_float(gv[e] << 16);
          s += __uint_as_float(xv[e] & 0xffff0000u) * __uint_as_float(gv[e] & 0xffff0000u);
        }
        acc += ok ? s : 0.f;
      }
#pragma unroll
      for (int o = 16; o > 0; o >>= 1) acc += __shfl_xor(acc, o);
      if (l5 == 0) p.gate[row] = 1.f / (1.f + __expf(-acc));
    }
    STAMP_OUT(0);
    return;
  }

  __builtin_amdgcn_s_setprio(2);
  const int w = wid - 4;
  const int g = lane >> 4, q = (lane & 15) >> 2, pq = lane & 3, li = lane & 15;
  const unsigned lrow = 16 * (g >> 1) + 4 * (g & 1) + q;
  char* const zb = (char*)p.z;
  if (is_attn) {
    unsigned wa[5];
#pragma unroll
    for (int i = 0; i < 4; ++i) wa[i] = lds0 + lrow * 480u + 40u * ((i + w) & 3) + 8u * pq;
    wa[4] = lds0 + lrow * 480u + 40u * pq + 32u;
    unsigned xa[2], xh[2];
#pragma unroll
    for (int ks = 0; ks < 2; ++ks) {
      unsigned sw = 16u * ((4 * ks + g) ^ (lane & 7));
      xh[ks] = lds0 + A_WB + li * 128u + sw;
      xa[ks] = xh[ks] + 2048u * (1 + 2 * w);
    }
    f32x4 acc[15][2], hk0 = {}, hkx = {}, hv0 = {}, hvx = {};
#pragma unroll
    for (int u = 0; u < 15; ++u) { acc[u][0] = f32x4{}; acc[u][1] = f32x4{}; }

    bf16x8 wn[6], xn[3];
    unsigned wc_[5], xc_[2], hc_[2];
#define A_RDW(u) lds_tr8(wc_[(u) % 5] + (32 * ((u) / 15)) * 480u + (((u) % 15) / 5) * 160u, wc_[(u) % 5] + (32 * ((u) / 15) + 8) * 480u + (((u) % 15) / 5) * 160u)
#define A_SETADDR(sb) do { _Pragma("unroll") for (int i = 0; i < 5; ++i) { wc_[i] = wa[i] + (sb); OPAQUE(wc_[i]); } \
      _Pragma("unroll") for (int i = 0; i < 2; ++i) { xc_[i] = xa[i] + (sb); hc_[i] = xh[i] + (sb); OPAQUE(xc_[i]); OPAQUE(hc_[i]); } } while (0)
    A_SETADDR(0u);
    LGKM_BAR();
    prog_wait_sleep(f_oth, 1);
    STAMP(1);
    xn[0] = lds_b128(xc_[0]); xn[1] = lds_b128(xc_[0] + 2048u);
#pragma unroll
    for (int i = 0; i < 6; ++i) wn[i] = A_RDW(i);
    xn[2] = lds_b128(hc_[0]);
    SB();
    unsigned sbn = A_STAGE;
#define A_ADR(u, t) (wc_[(u) % 5] + (32 * ((u) / 15) + 8 * (t)) * 480u + (((u) % 15) / 5) * 160u)
    for (int st = 0; st < 10; ++st) {
      bf16x8 wf[30], xf[2][3];
      unsigned seen = 0;
#pragma unroll
      for (int i = 0; i < 6; ++i) wf[i] = wn[i];
#pragma unroll
      for (int i = 0; i < 3; ++i) xf[0][i] = xn[i];
#pragma unroll
      for (int u = 0; u < 30; ++u) {
        const int ks = u / 15, uu = u % 15, sec = uu / 5, tu = (u + 6) % 30;
        if (u == 24) {
          prog_wait(f_oth, seen, st < 9 ? st + 2 : 0);
          STAMPL(2 + st);
          SB();
        }
        if (sec < 2) acc[uu][0] = mfma(wf[u], xf[ks][0], acc[uu][0]); else acc[uu][0] = mfma(xf[ks][0], wf[u], acc[uu][0]);
        SB();
        const bf16x4 lo = lds_tr4(A_ADR(tu, 0));
        SB();
        if (sec < 2) acc[uu][1] = mfma(wf[u], xf[ks][1], acc[uu][1]); else acc[uu][1] = mfma(xf[ks][1], wf[u], acc[uu][1]);
        SB();
        const bf16x4 hi = lds_tr4(A_ADR(tu, 1));
        if (u < 24) wf[u + 6] = cat8(lo, hi); else wn[u - 24] = cat8(lo, hi);
        if (u >= 19 && u < 24) { wc_[u - 19] = wa[u - 19] + sbn; OPAQUE(wc_[u - 19]); }
        if (u == 12) { xc_[0] = xa[0] + sbn; OPAQUE(xc_[0]); }
        if (u == 13) { hc_[0] = xh[0] + sbn; OPAQUE(hc_[0]); }
        if (u == 16) { xc_[1] = xa[1] + sbn; OPAQUE(xc_[1]); }
        if (u == 17) { hc_[1] = xh[1] + sbn; OPAQUE(hc_[1]); }
        if (u == 23) prog_set(f_own, st + 1);
        SB();
        if (uu == 5) hk0 = mfma(wf[u], xf[ks][2], hk0);
        if (uu == 9) hkx = mfma(wf[u], xf[ks][2], hkx);
        if (uu == 10) hv0 = mfma(xf[ks][2], wf[u], hv0);
        if (uu == 14) hvx = mfma(xf[ks][2], wf[u], hvx);
        SB();
        if (u == 5) xf[1][0] = lds_b128(xc_[1]);
        if (u == 9) xf[1][1] = lds_b128(xc_[1] + 2048u);
        if (u == 10) xf[1][2] = lds_b128(hc_[1]);
        if (u == 20) seen = prog_read(f_oth);
        if (u == 24) xn[0] = lds_b128(xc_[0]);
        if (u == 25) xn[1] = lds_b128(xc_[0] + 2048u);
        if (u == 29) { xn[2] = lds_b128(hc_[0]); sbn = sbn == 2 * A_STAGE ? 0u : sbn + A_STAGE; }
        SB();
      }
    }
    STAMP(12);
    const unsigned ex0 = lds0 + 2 * A_STAGE + lane * 16u;
    u32x2 Kp[5][2], Vp[5][2];
#pragma unroll
    for (int i = 0; i < 5; ++i)
#pragma unroll
      for (int bk = 0; bk < 2; ++bk) { Kp[i][bk] = pk4(acc[5 + i][bk]); Vp[i][bk] = pk4(acc[10 + i][bk]); }
    prog_wait(f_oth + 1024u, prog_read(f_oth + 1024u), 9);
    {
      if (w < 3) {
#pragma unroll
        for (int i = 0; i < 5; ++i) {
          unsigned slot = i < 4 ? ((i + w) & 3) : 4;
          u32x4 v = {Kp[i][1][0], Kp[i][1][1], Vp[i][1][0], Vp[i][1][1]};
          *(LAS u32x4*)(size_t)(ex0 + (w + 1) * 5120u + slot * 1024u) = v;
        }
      }
      u32x2 a = pk4(hk0), bb = pk4(hv0);
      u32x4 v = {a[0], a[1], bb[0], bb[1]};
      *(LAS u32x4*)(size_t)(ex0 + w * 1024u) = v;
      if (w == 0) {
        u32x2 a2 = pk4(hkx), b2 = pk4(hvx);
        u32x4 v2 = {a2[0], a2[1], b2[0], b2[1]};
        *(LAS u32x4*)(size_t)(ex0 + 4 * 1024u) = v2;
      }
    }
    const int par = li & 1;
    const bool cura = (4 * g + par) <= li, curb = (4 * g + par + 2) <= li;
    const unsigned hm = par ? 0xffff0000u : 0x0000ffffu;
    const unsigned ma0 = cura ? 0u : hm, ma1 = cura ? hm : 0u, mb0 = curb ? 0u : hm, mb1 = curb ? hm : 0u;
    const float SC = 0.22360679774997896f * 1.4426950408889634f, NEG = -1e9f * 1.4426950408889634f;
    const bool first = (rt == 0) && (w == 0);
    const unsigned vz = lane * 16u;
    u32x2 Kh[5], Vh[5];
    auto attend = [&](const int bk) {
      const bool fm = first && bk == 0;
      const float sca = (fm && !cura) ? 0.f : SC, bia = (fm && !cura) ? NEG : 0.f;
      const float scb = (fm && !curb) ? 0.f : SC, bib = (fm && !curb) ? NEG : 0.f;
      f32x4 ox = {};
      const int tb = rt * 8 + 2 * w + bk;
      const u32x2 Qx = pk4(acc[4][bk]);
#pragma unroll
      for (int i = 0; i < 4; ++i) {
        const int a = (i + w) & 3;
        const unsigned mq = (g == a) ? 0xffffffffu : 0u, mv = (q == a) ? 0xffffffffu : 0u;
        const u32x2 Qm = pk4(acc[i][bk]);
        const u32x4 Qf = {Qm[0], Qm[1], Qx[0] & mq, Qx[1] & mq};
        const u32x2 kp = bk == 0 ? Kh[i] : Kp[i][0], kxp = bk == 0 ? Kh[4] : Kp[4][0];
        const u32x4 Kprev = {kp[0], kp[1], kxp[0], kxp[1]};
        const u32x4 Kcur = {Kp[i][bk][0], Kp[i][bk][1], Kp[4][bk][0], Kp[4][bk][1]};
        f32x4 s0 = mfmau(Kprev, Qf, f32x4{}), s1 = mfmau(Kcur, Qf, f32x4{});
        float s0a = par ? s0[1] : s0[0], s0b = par ? s0[3] : s0[2];
        float s1a = par ? s1[1] : s1[0], s1b = par ? s1[3] : s1[2];
        float la = (cura ? s1a : s0a) * sca + bia, lb = (curb ? s1b : s0b) * scb + bib;
        float m = xg_max(vmax(la, lb));
        float ea = __builtin_amdgcn_exp2f(la - m), eb = __builtin_amdgcn_exp2f(lb - m);
        float inv = __builtin_amdgcn_rcpf(xg_sum(ea + eb));
        unsigned ta = pk2(ea * inv, ea * inv), tbb = pk2(eb * inv, eb * inv);
        const u32x4 P = {ta & ma0, tbb & mb0, ta & ma1, tbb & mb1};
        const u32x2 vprev = bk == 0 ? Vh[i] : Vp[i][0], vxp = bk == 0 ? Vh[4] : Vp[4][0];
        f32x4 om = mfmau(u32x4{vprev[0], vprev[1], Vp[i][bk][0], Vp[i][bk][1]}, P, f32x4{});
        ox = mfmau(u32x4{vxp[0] & mv, vxp[1] & mv, Vp[4][bk][0] & mv, Vp[4][bk][1] & mv}, P, ox);
        *(u32x2*)(zb + (size_t)((tb * KS2 + 2 * G + (a >> 1)) * 1024 + 8 * (a & 1)) + (size_t)vz) = pk4(om);
      }
      *(u32x2*)(zb + (size_t)((tb * KS2 + 16 + (G >> 1)) * 1024 + 8 * (G & 1)) + (size_t)vz) = pk4(ox);
    };
    attend(1);
    LGKM_BAR();
#pragma unroll
    for (int i = 0; i < 5; ++i) {
      unsigned slot = i < 4 ? ((i + w) & 3) : 4;
      u32x4 v = *(LAS u32x4*)(size_t)(ex0 + w * 5120u + slot * 1024u);
      Kh[i] = u32x2{v[0], v[1]}; Vh[i] = u32x2{v[2], v[3]};
    }
    attend(0);
  } else {
    const int wr = w >> 1, wc = w & 1;
    const unsigned f = 2 * (g & 1) + (q >> 1);
    unsigned wa[6];
#pragma unroll
    for (int n = 0; n < 6; ++n) wa[n] = lds0 + lrow * 384u + 32u * ((6 * wc + n) ^ f) + 8u * pq;
    unsigned xa[2];
#pragma unroll
    for (int ks = 0; ks < 2; ++ks) xa[ks] = lds0 + M_WB + (64 * wr + li) * 128u + 16u * ((4 * ks + g) ^ (lane & 7));
    f32x4 acc[6][4];
    {
      const int L = ((idx - 16) * 4 + w) * 36 + (lane < 36 ? lane : 0);
      const char* pa; unsigned stride;
      if (L < 528) { int r = (xcd >> 1) * 512 - 16 + L; r = r < 0 ? 0 : r; pa = (const char*)p.x + r * 1280; stride = 128u; }
      else if (L < 1488) { const int t = L - 528, k = t / 15, j = t % 15; pa = (const char*)p.wqkv + k * 3840 + (j / 5) * 1280 + ch * 640 + (j % 5) * 128; stride = 245760u; }
      else { const int t = (L < 2256 ? L : 1488) - 1488, k = t / 12, j = t % 12; pa = (const char*)p.w1 + k * 3072 + ch * 1536 + j * 128; stride = 196608u; }
      pa += 3 * (size_t)stride;
#pragma unroll
      for (int st = 3; st < 10; ++st) { touch(pa, lds0 + K1_FLAGS + 2048u); pa += stride; }
    }
    bf16x8 wn[3], xn[4];
    unsigned wc_[6], xc_[2];
#define M_RDW(u) lds_tr8(wc_[(u) % 6] + (32 * ((u) / 6)) * 384u, wc_[(u) % 6] + (32 * ((u) / 6) + 8) * 384u)
#define M_SETADDR(sb) do { _Pragma("unroll") for (int i = 0; i < 6; ++i) { wc_[i] = wa[i] + (sb); OPAQUE(wc_[i]); } \
      _Pragma("unroll") for (int i = 0; i < 2; ++i) { xc_[i] = xa[i] + (sb); OPAQUE(xc_[i]); } } while (0)
    M_SETADDR(0u);
    LGKM_BAR();
    prog_wait_sleep(f_oth, 1);
    STAMP(1);
    wn[0] = M_RDW(0);
#pragma unroll
    for (int i = 0; i < 4; ++i) xn[i] = lds_b128(xc_[0] + 2048u * i);
    wn[1] = M_RDW(1); wn[2] = M_RDW(2);
#pragma unroll
    for (int n = 0; n < 6; ++n) {
      f32x4 bi = bf4_to_f32(*(LAS u32x2*)(size_t)(lds0 + M_BIAS + (96 * wc + 16 * n + 4 * g) * 2));
#pragma unroll
      for (int m = 0; m < 4; ++m) acc[n][m] = bi;
    }
    SB();
    unsigned sbn = M_STAGE;
#define M_ADR(u, t) (wc_[(u) % 6] + (32 * ((u) / 6) + 8 * (t)) * 384u)
    for (int st = 0; st < 10; ++st) {
      bf16x8 wf[12], xf[2][4];
      unsigned seen = 0;
#pragma unroll
      for (int i = 0; i < 3; ++i) wf[i] = wn[i];
#pragma unroll
      for (int i = 0; i < 4; ++i) xf[0][i] = xn[i];
#pragma unroll
      for (int u = 0; u < 12; ++u) {
        const int ks = u / 6, n = u % 6, tu = (u + 3) % 12;
        if (u == 8) {
          prog_wait(f_oth, seen, st < 9 ? st + 2 : 0);
          STAMPL(2 + st);
          SB();
        }
        acc[n][0] = mfma(wf[u], xf[ks][0], acc[n][0]);
        SB();
        const bf16x4 lo = lds_tr4(M_ADR(tu, 0));
        SB();
        acc[n][1] = mfma(wf[u], xf[ks][1], acc[n][1]);
        SB();
        const bf16x4 hi = lds_tr4(M_ADR(tu, 1));
        if (u < 9) wf[u + 3] = cat8(lo, hi); else wn[u - 9] = cat8(lo, hi);
        SB();
        acc[n][2] = mfma(wf[u], xf[ks][2], acc[n][2]);
        SB();
        if (u < 4) xf[1][u] = lds_b128(xc_[1] + 2048u * u);
        if (u == 5) seen = prog_read(f_oth);
        if (u >= 8) xn[u - 8] = lds_b128(xc_[0] + 2048u * (u - 8));
        SB();
        acc[n][3] = mfma(wf[u], xf[ks][3], acc[n][3]);
        SB();
        if (u >= 3 && u < 9) { wc_[u - 3] = wa[u - 3] + sbn; OPAQUE(wc_[u - 3]); }
        if (u == 1) { xc_[0] = xa[0] + sbn; OPAQUE(xc_[0]); }
        if (u == 9) { xc_[1] = xa[1] + sbn; OPAQUE(xc_[1]); }
        if (u == 8) prog_set(f_own, st + 1);
        if (u == 11) sbn = sbn == 2 * M_STAGE ? 0u : sbn + M_STAGE;
        SB();
      }
    }
    STAMP(12);
    const float C1 = -2.f * 0.7978845608028654f * 1.4426950408889634f, C2 = C1 * 0.044715f;
    const unsigned vz = lane * 16u;
#pragma unroll
    for (int m = 0; m < 4; ++m) {
      const int tb = rt * 8 + 4 * wr + m;
#pragma unroll
      for (int j = 0; j < 3; ++j) {
        u32x4 o;
#pragma unroll
        for (int e = 0; e < 2; ++e) {
#pragma unroll
          for (int k = 0; k < 2; ++k) {
            f32x2 x = {acc[2 * j + e][m][2 * k], acc[2 * j + e][m][2 * k + 1]};
            f32x2 t = x * (x * x * C2 + C1);
            f32x2 d = {__builtin_amdgcn_exp2f(t[0]), __builtin_amdgcn_exp2f(t[1])};
            d = d + 1.f;
            f32x2 r = {__builtin_amdgcn_rcpf(d[0]), __builtin_amdgcn_rcpf(d[1])};
            r = r * x;
            o[2 * e + k] = pk2(r[0], r[1]);
          }
        }
        *(u32x4*)(zb + (size_t)((tb * KS2 + 20 + 6 * G + 3 * wc + j) * 1024) + (size_t)vz) = o;
      }
    }
  }
  STAMP(13);
  STAMP_OUT(0);
}

constexpr unsigned K2_STAGE = 20480, K2_NB = 7, K2_FLAGS = K2_STAGE * K2_NB, K2_GB = K2_FLAGS + 2048 + 256, K2_LDS = K2_GB + 2048;
struct K2Args { const void* z; const void* wo; const void* w2; const void* b2; const float* gate; void* out; };

__global__ __launch_bounds__(512, 2) void k2(K2Args p) {
  extern __shared__ __attribute__((aligned(16))) char shm[];
  const unsigned lds0 = (unsigned)(uintptr_t)(LAS char*)shm;
  const int lane = threadIdx.x & 63;
  const int wid = __builtin_amdgcn_readfirstlane(threadIdx.x >> 6);
  const int b = blockIdx.x, xcd = b & 7, idx = b >> 3;
  const int rq = xcd >> 1, ch = xcd & 1;
  const int rti = idx % 6, cn = 5 * ch + idx / 6;
  const int tb0 = rq * 32 + rti * 6;
  const int nblk = rti < 5 ? 6 : 2;
  const unsigned f_own = lds0 + K2_FLAGS + wid * 256u + lane * 4u;
  const unsigned f_oth = lds0 + K2_FLAGS + (wid < 4 ? 1024u : 0u) + (lane & 3) * 260u;
  prog_set(f_own, 0u);
  STAMP_DECL
  STAMP(0); STAMP_RT(61);

  if (wid < 4) {
    const int lw = wid;
    const srd_t sz = make_srd(p.z, Z_BYTES);
    const srd_t so = make_srd(p.wo, D_ * D_ * 2);
    const srd_t s2 = make_srd(p.w2, F_ * D_ * 2);
    const unsigned rho = 8 * lw + (lane >> 3), t = rho >> 4, gg = (rho >> 2) & 3, qq = rho & 3;
    const unsigned cpos = lane & 7, sch = (((cpos >> 1) ^ ((rho >> 1) & 3)) << 1) | (cpos & 1);
    const unsigned cb = 128u * cn + 16u * sch;
    const unsigned vA = (20 * t + (rho & 15)) * 1280u + cb;
    const unsigned vB = (80 * t + 20 * gg + 16 + qq) * 1280u + cb;
    const unsigned vC = rho * 1280u + cb;
    const unsigned vz = lane * 16u;
    unsigned zo[3];
#pragma unroll
    for (int i = 0; i < 3; ++i) { int n = 3 * lw + i, blk = n >> 1; blk = blk < nblk ? blk : 0; zo[i] = (unsigned)(((tb0 + blk) * KS2 + (n & 1)) * 1024); }
    const srd_t sq = make_srd(g_seq, 256);
    const unsigned vzero = 0, f_row = lds0 + K2_FLAGS + wid * 256u;
    auto issue = [&](int st, unsigned lb, srd_t sw, unsigned vw, unsigned wo0, unsigned wstep) {
#pragma unroll
      for (int i = 0; i < 3; ++i) dma16(vz, sz, zo[i] + 2048u * st, lb + 1024u * (3 * lw + i));
      dma16(vw, sw, wo0, lb + 12288u + 1024u * lw);
      dma16(vw, sw, wo0 + wstep, lb + 16384u + 1024u * lw);
    };
    STAMP(1);
    issue(0, lds0, so, vA, 0u, 51200u);
    if (lw == 0) dma16(vz, make_srd(p.gate, S_ * 4), tb0 * 64u, lds0 + K2_GB);
    if (lw == 1) dma16(vz, make_srd(p.b2, D_ * 2), cn * 128u, lds0 + K2_GB + 1024u);
    LGKM_BAR();
    dma4(vzero, sq, 4, f_row);
#pragma unroll
    for (int st = 1; st < 7; ++st) { issue(st, lds0 + st * K2_STAGE, so, vA, 102400u * st, 51200u); dma4(vzero, sq, 4 * st + 4, f_row); }
    prog_wait_sleep(f_oth, 1);
    issue(7, lds0, so, vA, 102400u * 7, 51200u); dma4(vzero, sq, 32, f_row);
    prog_wait_sleep(f_oth, 2);
    issue(8, lds0 + K2_STAGE, so, vB, 0u, 204800u); dma4(vzero, sq, 36, f_row);
    prog_wait_sleep(f_oth, 3);
    issue(9, lds0 + 2 * K2_STAGE, so, vB, 409600u, 204800u); dma4(vzero, sq, 40, f_row);
    unsigned lb = lds0 + 3 * K2_STAGE;
    unsigned seen = prog_read(f_oth);
    for (int st = 10; st < 34; ++st) {
      if (__builtin_amdgcn_ballot_w64(seen < (unsigned)(st - (K2_NB - 1))) != 0) prog_wait_sleep(f_oth, st - (K2_NB - 1));
      seen = prog_read(f_oth);
      ISS(issue(SST(st), lb, s2, vC, 81920u * SST(st - 10), 40960u));
      dma4(vzero, sq, 4 * st + 4, f_row);
      STAMPL(st);
      lb = lb == lds0 + (K2_NB - 1) * K2_STAGE ? lds0 : lb + K2_STAGE;
    }
    STAMP(40);
    STAMP_OUT(1);
    return;
  }
  __builtin_amdgcn_s_setprio(2);
  const int w = wid - 4, wr = w >> 1, wc = w & 1;
  const int g = lane >> 4, q = (lane & 15) >> 2, pq = lane & 3, li = lane & 15;
  const unsigned za = lds0 + wr * 3 * 2048u + lane * 16u;
  unsigned wa[2];
  {
    const unsigned r = 4 * g + q;
#pragma unroll
    for (int n = 0; n < 2; ++n) wa[n] = lds0 + 12288u + r * 128u + 32u * ((2 * wc + n) ^ ((r >> 1) & 3)) + 8u * pq;
  }
  f32x4 acc[2][2][3];
#pragma unroll
  for (int h = 0; h < 2; ++h)
#pragma unroll
    for (int n = 0; n < 2; ++n)
#pragma unroll
      for (int m = 0; m < 3; ++m) acc[h][n][m] = f32x4{};
  bf16x8 zf[2][2][3], wf[2][2][2];
  unsigned zc = za, wc0 = wa[0], wc1 = wa[1], sbn = K2_STAGE;
  LGKM_BAR();
  prog_wait_sleep(f_oth, 1);
  STAMP(1);
  unsigned seen = prog_read(f_oth);
  SB();
#pragma unroll
  for (int e = 0; e < 2; ++e) {
#pragma unroll
    for (int m = 0; m < 3; ++m) zf[0][e][m] = lds_b128(zc + 2048u * m + 1024u * e);
    wf[0][e][0] = lds_tr8(wc0 + 4096u * e, wc0 + 4096u * e + 2048u);
    wf[0][e][1] = lds_tr8(wc1 + 4096u * e, wc1 + 4096u * e + 2048u);
  }
  prog_set(f_own, 1);
  auto stage = [&](const int st, const int pp, f32x4 (&a)[2][3]) {
    prog_wait(f_oth, seen, st < 33 ? st + 2 : 0);
    STAMPL(2 + st);
    SB();
    bf16x4 lo, hi;
#pragma unroll
    for (int e = 0; e < 2; ++e) {
      const unsigned eo = 4096u * e;
      MF(a[0][0], wf[pp][e][0], zf[pp][e][0]); SB();
      lo = lds_tr4(wc0 + eo); hi = lds_tr4(wc0 + eo + 2048u); wf[pp ^ 1][e][0] = cat8(lo, hi); SB();
      MF(a[0][1], wf[pp][e][0], zf[pp][e][1]); SB();
      zf[pp ^ 1][e][0] = lds_b128(zc + 1024u * e); SB();
      MF(a[0][2], wf[pp][e][0], zf[pp][e][2]); SB();
      zf[pp ^ 1][e][1] = lds_b128(zc + 2048u + 1024u * e); SB();
      MF(a[1][0], wf[pp][e][1], zf[pp][e][0]); SB();
      zf[pp ^ 1][e][2] = lds_b128(zc + 4096u + 1024u * e); SB();
      MF(a[1][1], wf[pp][e][1], zf[pp][e][1]); SB();
      lo = lds_tr4(wc1 + eo); hi = lds_tr4(wc1 + eo + 2048u); wf[pp ^ 1][e][1] = cat8(lo, hi); SB();
      MF(a[1][2], wf[pp][e][1], zf[pp][e][2]); SB();
      if (e == 0) seen = prog_read(f_oth);
      else {
        prog_set(f_own, st + 2);
        zc = za + sbn; wc0 = wa[0] + sbn; wc1 = wa[1] + sbn;
        OPAQUE(zc); OPAQUE(wc0); OPAQUE(wc1);
        sbn = sbn == (K2_NB - 1) * K2_STAGE ? 0u : sbn + K2_STAGE;
      }
      SB();
    }
  };
  zc = za + K2_STAGE; wc0 = wa[0] + K2_STAGE; wc1 = wa[1] + K2_STAGE; sbn = 2 * K2_STAGE;
  for (int st = 0; st < 10; st += 2) { stage(st, 0, acc[0]); stage(st + 1, 1, acc[0]); }
  for (int st = 10; st < 34; st += 2) { stage(st, 0, acc[1]); stage(st + 1, 1, acc[1]); }
  asm volatile("s_nop 7\n\ts_nop 7" ::: "memory");
  SB();
  STAMP(40);
  float gt[3];
  u32x2 bv[2];
#pragma unroll
  for (int m = 0; m < 3; ++m) gt[m] = *(LAS float*)(size_t)(lds0 + K2_GB + ((3 * wr + m) * 16 + li) * 4);
#pragma unroll
  for (int n = 0; n < 2; ++n) bv[n] = *(LAS u32x2*)(size_t)(lds0 + K2_GB + 1024u + (32 * wc + 16 * n + 4 * g) * 2);
#pragma unroll
  for (int m = 0; m < 3; ++m) {
    const int blk = 3 * wr + m;
    if (blk < nblk) {
      const int tok = (tb0 + blk) * 16 + li;
#pragma unroll
      for (int n = 0; n < 2; ++n) {
        const int col = 64 * cn + 32 * wc + 16 * n + 4 * g;
        f32x4 r = acc[0][n][m] * gt[m] + (acc[1][n][m] + bf4_to_f32(bv[n])) * (1.f - gt[m]);
        *(u32x2*)((char*)p.out + ((size_t)tok * D_ + col) * 2) = pk4(r);
      }
    }
  }
  STAMP(41);
  STAMP_OUT(1);
}

extern "C" void kernel_launch(void* const* d_in, const int* in_sizes, int n_in, void* d_out, int out_size, void* d_ws,
                              size_t ws_size, hipStream_t stream) {
  (void)in_sizes; (void)n_in; (void)out_size; (void)ws_size;
  (void)hipFuncSetAttribute((const void*)k1, hipFuncAttributeMaxDynamicSharedMemorySize, K1_LDS);
  (void)hipFuncSetAttribute((const void*)k2, hipFuncAttributeMaxDynamicSharedMemorySize, K2_LDS);
  char* ws = (char*)d_ws;
  K1Args a1{d_in[0], d_in[1], d_in[4], d_in[5], d_in[8], ws, (float*)(ws + Z_BYTES), d_in[2], d_in[6], d_out, d_in[7]};
  K2Args a2{ws, d_in[2], d_in[6], d_in[7], (const float*)(ws + Z_BYTES), d_out};
  k1<<<256, 512, K1_LDS, stream>>>(a1);
  k2<<<240, 512, K2_LDS, stream>>>(a2);
}
```

```cpp
#define AMP_K 0
#define AMP_BLK 0
#define AMP_W 0
#define AMP_A 0
#define AMP_B 1
#include <hip/hip_runtime.h>
#include <hip/hip_bf16.h>
#include <stdint.h>

typedef __attribute__((ext_vector_type(8))) __bf16 bf16x8;
typedef __attribute__((ext_vector_type(4))) __bf16 bf16x4;
typedef __attribute__((ext_vector_type(2))) __bf16 bf16x2;
typedef __attribute__((ext_vector_type(4))) float f32x4;
typedef __attribute__((ext_vector_type(2))) float f32x2;
typedef __attribute__((ext_vector_type(4))) unsigned u32x4;
typedef __attribute__((ext_vector_type(2))) unsigned u32x2;
typedef __attribute__((ext_vector_type(8))) unsigned u32x8;
typedef __amdgpu_buffer_rsrc_t srd_t;
#define LAS __attribute__((address_space(3)))
#define DI __device__ __forceinline__

constexpr int S_ = 2048, D_ = 640, F_ = 1536;
constexpr int KS2 = 68;
constexpr unsigned Z_BYTES = 128u * KS2 * 1024u;

DI srd_t make_srd(const void* p, unsigned bytes) {
  return __builtin_amdgcn_make_buffer_rsrc((void*)p, (short)0, (int)bytes, 0x00020000);
}
DI void dma16(unsigned voff, srd_t srd, unsigned soff, unsigned lds) {
  asm volatile("s_mov_b32 m0, %0\n\ts_nop 0\n\tbuffer_load_dwordx4 %1, %2, %3 offen lds"
               :: "s"(lds), "v"(voff), "s"(srd), "s"(soff) : "memory");
}
__device__ const unsigned g_seq[64] = {0,  1,  2,  3,  4,  5,  6,  7,  8,  9,  10, 11, 12, 13, 14, 15, 16, 17, 18, 19, 20, 21,
                                       22, 23, 24, 25, 26, 27, 28, 29, 30, 31, 32, 33, 34, 35, 36, 37, 38, 39, 40, 41, 42, 43,
                                       44, 45, 46, 47, 48, 49, 50, 51, 52, 53, 54, 55, 56, 57, 58, 59, 60, 61, 62, 63};
DI void dma4(unsigned voff, srd_t srd, unsigned soff, unsigned lds) {
  asm volatile("s_mov_b32 m0, %0\n\ts_nop 0\n\tbuffer_load_dword %1, %2, %3 offen lds"
               :: "s"(lds), "v"(voff), "s"(srd), "s"(soff) : "memory");
}
DI void touch(const char* addr, unsigned lds_scratch) {
  asm volatile("s_mov_b32 m0, %0\n\ts_nop 0\n\tglobal_load_lds_dword %1, off" :: "s"(lds_scratch), "v"(addr) : "memory");
}
template <unsigned HI> DI void dma16m(unsigned voff, srd_t srd, unsigned soff, unsigned lds) {
  asm volatile("s_mov_b32 m0, %0\n\ts_mov_b32 exec_hi, %4\n\tbuffer_load_dwordx4 %1, %2, %3 offen lds\n\ts_mov_b32 exec_hi, -1"
               :: "s"(lds), "v"(voff), "s"(srd), "s"(soff), "i"(HI) : "memory");
}
DI bf16x8 lds_tr8(unsigned a0, unsigned a1) {
  bf16x4 lo = __builtin_amdgcn_ds_read_tr16_b64_v4bf16((LAS bf16x4*)(size_t)a0);
  bf16x4 hi = __builtin_amdgcn_ds_read_tr16_b64_v4bf16((LAS bf16x4*)(size_t)a1);
  return __builtin_shufflevector(lo, hi, 0, 1, 2, 3, 4, 5, 6, 7);
}
DI bf16x4 lds_tr4(unsigned a) { return __builtin_amdgcn_ds_read_tr16_b64_v4bf16((LAS bf16x4*)(size_t)a); }
DI bf16x8 cat8(bf16x4 lo, bf16x4 hi) { return __builtin_shufflevector(lo, hi, 0, 1, 2, 3, 4, 5, 6, 7); }
DI bf16x8 lds_b128(unsigned a) { return *(LAS bf16x8*)(size_t)a; }
DI f32x4 mfma(bf16x8 a, bf16x8 b, f32x4 c) { return __builtin_amdgcn_mfma_f32_16x16x32_bf16(a, b, c, 0, 0, 0); }
DI f32x4 mfmau(u32x4 a, u32x4 b, f32x4 c) { return mfma(__builtin_bit_cast(bf16x8, a), __builtin_bit_cast(bf16x8, b), c); }
DI unsigned pk2(float a, float b) { bf16x2 v = {(__bf16)a, (__bf16)b}; return __builtin_bit_cast(unsigned, v); }
DI u32x2 pk4(f32x4 v) { u32x2 r = {pk2(v[0], v[1]), pk2(v[2], v[3])}; return r; }
DI float vmax(float a, float b) { return __builtin_amdgcn_fmed3f(a, b, __builtin_inff()); }
DI float xg_max(float x) {
  auto s = __builtin_amdgcn_permlane16_swap(__float_as_uint(x), __float_as_uint(x), false, false);
  x = vmax(__uint_as_float(s[0]), __uint_as_float(s[1]));
  auto t = __builtin_amdgcn_permlane32_swap(__float_as_uint(x), __float_as_uint(x), false, false);
  return vmax(__uint_as_float(t[0]), __uint_as_float(t[1]));
}
DI float xg_sum(float x) {
  auto s = __builtin_amdgcn_permlane16_swap(__float_as_uint(x), __float_as_uint(x), false, false);
  x = __uint_as_float(s[0]) + __uint_as_float(s[1]);
  auto t = __builtin_amdgcn_permlane32_swap(__float_as_uint(x), __float_as_uint(x), false, false);
  return __uint_as_float(t[0]) + __uint_as_float(t[1]);
}
DI f32x4 bf4_to_f32(u32x2 v) {
  return f32x4{__uint_as_float(v[0] << 16), __uint_as_float(v[0] & 0xffff0000u), __uint_as_float(v[1] << 16), __uint_as_float(v[1] & 0xffff0000u)};
}
#define STAMP_DECL
#define STAMP(slot)
#define STAMPL(slot)
#define STAMP_RT(slot)
#define STAMP_OUT(kid)
#define SST(x) (x)
#define ISS(x) x
#define MF(c, a, b) asm volatile("v_mfma_f32_16x16x32_bf16 %0, %1, %2, %0" : "+v"(c) : "v"(a), "v"(b))
#undef STAMP_DECL
#undef STAMP
#undef STAMP_OUT
#define STAMP_DECL unsigned long long amp_a = 0, amp_b = 0; const int amp_slot_out = 62;
#define STAMP(slot) do{ if ((slot) == AMP_A || (slot) == AMP_B) { unsigned long long _c; asm volatile("s_memrealtime %0\n\ts_waitcnt lgkmcnt(0)" : "=s"(_c) :: "memory"); if ((slot) == AMP_A) amp_a = _c; else amp_b = _c; } }while(0)
#define STAMP_OUT(kid) do{ asm volatile("s_waitcnt vmcnt(0)":::"memory"); STAMP(62); \
   if ((kid) == AMP_K && blockIdx.x == AMP_BLK && wid == AMP_W && lane == 0) *(unsigned*)((char*)p.gate + 16384) = (unsigned)(amp_b - amp_a); \
   if ((kid) == 1 && blockIdx.x == AMP_BLK && wid == AMP_W) { unsigned d = AMP_K == 1 ? (unsigned)(amp_b - amp_a) : __builtin_amdgcn_readfirstlane(*(const unsigned*)((const char*)p.gate + 16384)); \
      unsigned long long t0 = __builtin_amdgcn_s_memrealtime(); d = d > 3000u ? 3000u : d; while (__builtin_amdgcn_s_memrealtime() - t0 < 10ull * d) {} } }while(0)
#define SB() __builtin_amdgcn_sched_barrier(0)
#define OPAQUE(x) asm volatile("" : "+v"(x))
#define WAITV(N) asm volatile("s_waitcnt vmcnt(" #N ")" ::: "memory")
#define LGKM_BAR() asm volatile("s_waitcnt lgkmcnt(0)\n\ts_barrier" ::: "memory")
#define FENCE() asm volatile("" ::: "memory")

DI void prog_set(unsigned row_addr  , unsigned v) {
  FENCE();
  __hip_atomic_store((LAS unsigned*)(size_t)row_addr, v, __ATOMIC_RELAXED, __HIP_MEMORY_SCOPE_WORKGROUP);
  FENCE();
}
DI unsigned prog_read(unsigned addr  ) { return __hip_atomic_load((LAS unsigned*)(size_t)addr, __ATOMIC_RELAXED, __HIP_MEMORY_SCOPE_WORKGROUP); }
DI void prog_wait(unsigned addr, unsigned seen, unsigned need) {
  if (__builtin_expect(__builtin_amdgcn_ballot_w64(seen < need) != 0, 0)) {
    unsigned long long m;
    do {
      unsigned v;
      asm volatile("ds_read_b32 %0, %1\n\ts_waitcnt lgkmcnt(0)" : "=v"(v) : "v"(addr) : "memory");
      m = __builtin_amdgcn_ballot_w64(v < need);
    } while (m != 0);
  }
  FENCE();
}
DI void prog_wait_sleep(unsigned addr, unsigned need) {
  while (__builtin_amdgcn_ballot_w64(prog_read(addr) < need) != 0) __builtin_amdgcn_s_sleep(1);
  FENCE();
}

DI unsigned swz23(unsigned k) { return (k & ~12u) | ((k & 4u) << 1) | ((k & 8u) >> 1); }

constexpr unsigned A_WB = 30720, A_STAGE = 49152;
constexpr unsigned M_WB = 24576, M_STAGE = 40960;
constexpr unsigned M_BIAS = 3 * M_STAGE;
constexpr unsigned K1_FLAGS = 3 * A_STAGE, K1_LDS = K1_FLAGS + 2048 + 256;

struct K1Args { const void* x; const void* wqkv; const void* w1; const void* b1; const void* gw; void* z; float* gate; const void* wo; const void* w2; const void* out; const void* b2; };

__global__ __launch_bounds__(512, 2) void k1(K1Args p) {
  extern __shared__ __attribute__((aligned(16))) char shm[];
  const unsigned lds0 = (unsigned)(uintptr_t)(LAS char*)shm;
  const int lane = threadIdx.x & 63;
  const int wid = __builtin_amdgcn_readfirstlane(threadIdx.x >> 6);
  const int b = blockIdx.x, xcd = b & 7, idx = b >> 3;
  const int rt = (xcd >> 1) * 4 + (idx & 3), c = idx >> 2, ch = xcd & 1;
  const bool is_attn = c < 4;
  const int G = 4 * ch + (c & 3);
  const int T0 = rt * 128;
  const unsigned f_own = lds0 + K1_FLAGS + wid * 256u + lane * 4u;
  const unsigned f_oth = lds0 + K1_FLAGS + (wid < 4 ? 1024u : 0u) + (lane & 3) * 260u;
  prog_set(f_own, 0u);
  STAMP_DECL
  STAMP(0); STAMP_RT(61);

  if (wid < 4) {
    const int lw = wid;
    const srd_t sx = make_srd(p.x, S_ * D_ * 2);
    const srd_t sq = make_srd(g_seq, 256);
    const unsigned vzero = 0, f_row = lds0 + K1_FLAGS + wid * 256u;
    const unsigned vx = (lane >> 3) * 1280u + 16u * ((lane & 7) ^ (lane >> 3));
    if (is_attn) {
      const srd_t sw = make_srd(p.wqkv, D_ * 3 * D_ * 2);
      const unsigned r2 = lane >= 30 ? 1u : 0u, rem = lane - 30u * r2, sec = (rem >= 20 ? 2u : (rem >= 10 ? 1u : 0u));
      const unsigned vw = r2 * 3840u + sec * 1280u + 16u * (rem - 10u * sec);
      const int hr = 4 * lw + (lane >> 3);
      const int ht = T0 - 16 + hr;
      const unsigned vh = (unsigned)(ht < 0 ? 0 : ht) * 1280u + 16u * ((lane & 7) ^ (hr & 7));
      auto issueA = [&](int st, unsigned lb) {
        const unsigned sow = st * 245760u + 160u * G, sox = st * 128u + (unsigned)T0 * 1280u;
        dma16m<0u>(vh, sx, st * 128u, lb + A_WB + 512u * lw);
#pragma unroll
        for (int i = 0; i < 4; ++i) { const unsigned n = 4 * lw + i; dma16(vx, sx, sox + n * 10240u, lb + A_WB + 2048u + 1024u * n); }
#pragma unroll
        for (int i = 0; i < 2; ++i) { const unsigned n = 8 * lw + i; dma16m<0x0fffffffu>(vw, sw, sow + swz23(2 * n) * 3840u, lb + 960u * n); }
      };
      auto issueB = [&](int st, unsigned lb) {
        const unsigned sow = st * 245760u + 160u * G;
#pragma unroll
        for (int i = 2; i < 8; ++i) { const unsigned n = 8 * lw + i; dma16m<0x0fffffffu>(vw, sw, sow + swz23(2 * n) * 3840u, lb + 960u * n); }
      };
      STAMP(1);
      issueA(0, lds0); issueB(0, lds0);
      LGKM_BAR();
      dma4(vzero, sq, 4, f_row);
      issueA(1, lds0 + A_STAGE); issueB(1, lds0 + A_STAGE); dma4(vzero, sq, 8, f_row);
      issueA(2, lds0 + 2 * A_STAGE); issueB(2, lds0 + 2 * A_STAGE); dma4(vzero, sq, 12, f_row);
      unsigned lb = lds0;
      for (int st = 3; st < 10; ++st) {
        prog_wait_sleep(f_oth, st - 2);
        ISS(issueA(SST(st), lb));
        ISS(issueB(SST(st), lb));
        dma4(vzero, sq, 4 * st + 4, f_row);
        STAMPL(1 + st);
        lb = lb == lds0 + 2 * A_STAGE ? lds0 : lb + A_STAGE;
      }
    } else {
      const srd_t sw = make_srd(p.w1, D_ * F_ * 2);
      unsigned vw[3];
#pragma unroll
      for (int i = 0; i < 3; ++i) {
        const unsigned cw = 64 * i + lane, j = (cw * 171u) >> 12, cp = cw - 24u * j;
        vw[i] = (8u * (j >> 2) + (j & 3)) * 3072u + 32u * ((cp >> 1) ^ ((j >> 1) & 3)) + 16u * (cp & 1);
      }
      auto issueA = [&](int st, unsigned lb) {
        const unsigned sow = st * 196608u + 384u * G, sox = st * 128u + (unsigned)T0 * 1280u;
#pragma unroll
        for (int i = 0; i < 4; ++i) { const unsigned n = 4 * lw + i; dma16(vx, sx, sox + n * 10240u, lb + M_WB + 1024u * n); }
#pragma unroll
        for (int i = 0; i < 3; ++i) dma16(vw[i], sw, sow + (16u * (lw >> 1) + 4u * (lw & 1)) * 3072u, lb + 3072u * lw + 1024u * i);
      };
      auto issueB = [&](int st, unsigned lb) {
        const unsigned sow = st * 196608u + 384u * G;
#pragma unroll
        for (int i = 0; i < 3; ++i) dma16(vw[i], sw, sow + (32u + 16u * (lw >> 1) + 4u * (lw & 1)) * 3072u, lb + 3072u * (lw + 4) + 1024u * i);
      };
      STAMP(1);
      issueA(0, lds0); issueB(0, lds0);
      if (lw == 0) dma16(lane * 16u, make_srd(p.b1, F_ * 2), 384u * G, lds0 + M_BIAS);
      LGKM_BAR();
      dma4(vzero, sq, 4, f_row);
      issueA(1, lds0 + M_STAGE); issueB(1, lds0 + M_STAGE); dma4(vzero, sq, 8, f_row);
      issueA(2, lds0 + 2 * M_STAGE); issueB(2, lds0 + 2 * M_STAGE); dma4(vzero, sq, 12, f_row);
      unsigned lb = lds0;
      for (int st = 3; st < 10; ++st) {
        prog_wait_sleep(f_oth, st - 2);
        ISS(issueA(SST(st), lb));
        ISS(issueB(SST(st), lb));
        dma4(vzero, sq, 4 * st + 4, f_row);
        STAMPL(1 + st);
        lb = lb == lds0 + 2 * M_STAGE ? lds0 : lb + M_STAGE;
      }
#pragma unroll
      for (int j = 0; j < 3; ++j) {
        int L = (j * 64 + (idx - 16) * 4 + lw) * 64 + lane;
        L = L < 10880 ? L : 0;
        const int r = L / 5, cl = L % 5;
        const char* pa = (r < 640 ? (const char*)p.wo + r * 1280 : (const char*)p.w2 + (r - 640) * 1280) + ch * 640 + cl * 128;
        touch(pa, lds0 + K1_FLAGS + 2048u);
      }
    }
    STAMP(20);
    {
      const int cc = is_attn ? G : 8 + G;
      const int row = T0 + 8 * cc + 2 * lw + (lane >> 5);
      const int l5 = lane & 31;
      const u32x4* xr = (const u32x4*)((const char*)p.x + (size_t)row * 1280);
      const u32x4* gr = (const u32x4*)p.gw;
      float acc = 0.f;
#pragma unroll
      for (int j = 0; j < 3; ++j) {
        int ck = l5 + 32 * j;
        bool ok = ck < 80;
        int ckc = ok ? ck : 0;
        u32x4 xv = xr[ckc], gv = gr[ckc];
        float s = 0.f;
#pragma unroll
        for (int e = 0; e < 4; ++e) {
          s += __uint_as_float(xv[e] << 16) * __uint_as_float(gv[e] << 16);
          s += __uint_as_float(xv[e] & 0xffff0000u) * __uint_as_float(gv[e] & 0xffff0000u);
        }
        acc += ok ? s : 0.f;
      }
#pragma unroll
      for (int o = 16; o > 0; o >>= 1) acc += __shfl_xor(acc, o);
      if (l5 == 0) p.gate[row] = 1.f / (1.f + __expf(-acc));
    }
    STAMP_OUT(0);
    return;
  }

  __builtin_amdgcn_s_setprio(2);
  const int w = wid - 4;
  const int g = lane >> 4, q = (lane & 15) >> 2, pq = lane & 3, li = lane & 15;
  const unsigned lrow = 16 * (g >> 1) + 4 * (g & 1) + q;
  char* const zb = (char*)p.z;
  if (is_attn) {
    unsigned wa[5];
#pragma unroll
    for (int i = 0; i < 4; ++i) wa[i] = lds0 + lrow * 480u + 40u * ((i + w) & 3) + 8u * pq;
    wa[4] = lds0 + lrow * 480u + 40u * pq + 32u;
    unsigned xa[2], xh[2];
#pragma unroll
    for (int ks = 0; ks < 2; ++ks) {
      unsigned sw = 16u * ((4 * ks + g) ^ (lane & 7));
      xh[ks] = lds0 + A_WB + li * 128u + sw;
      xa[ks] = xh[ks] + 2048u * (1 + 2 * w);
    }
    f32x4 acc[15][2], hk0 = {}, hkx = {}, hv0 = {}, hvx = {};
#pragma unroll
    for (int u = 0; u < 15; ++u) { acc[u][0] = f32x4{}; acc[u][1] = f32x4{}; }

    bf16x8 wn[6], xn[3];
    unsigned wc_[5], xc_[2], hc_[2];
#define A_RDW(u) lds_tr8(wc_[(u) % 5] + (32 * ((u) / 15)) * 480u + (((u) % 15) / 5) * 160u, wc_[(u) % 5] + (32 * ((u) / 15) + 8) * 480u + (((u) % 15) / 5) * 160u)
#define A_SETADDR(sb) do { _Pragma("unroll") for (int i = 0; i < 5; ++i) { wc_[i] = wa[i] + (sb); OPAQUE(wc_[i]); } \
      _Pragma("unroll") for (int i = 0; i < 2; ++i) { xc_[i] = xa[i] + (sb); hc_[i] = xh[i] + (sb); OPAQUE(xc_[i]); OPAQUE(hc_[i]); } } while (0)
    A_SETADDR(0u);
    LGKM_BAR();
    prog_wait_sleep(f_oth, 1);
    STAMP(1);
    xn[0] = lds_b128(xc_[0]); xn[1] = lds_b128(xc_[0] + 2048u);
#pragma unroll
    for (int i = 0; i < 6; ++i) wn[i] = A_RDW(i);
    xn[2] = lds_b128(hc_[0]);
    SB();
    unsigned sbn = A_STAGE;
#define A_ADR(u, t) (wc_[(u) % 5] + (32 * ((u) / 15) + 8 * (t)) * 480u + (((u) % 15) / 5) * 160u)
    for (int st = 0; st < 10; ++st) {
      bf16x8 wf[30], xf[2][3];
      unsigned seen = 0;
#pragma unroll
      for (int i = 0; i < 6; ++i) wf[i] = wn[i];
#pragma unroll
      for (int i = 0; i < 3; ++i) xf[0][i] = xn[i];
#pragma unroll
      for (int u = 0; u < 30; ++u) {
        const int ks = u / 15, uu = u % 15, sec = uu / 5, tu = (u + 6) % 30;
        if (u == 24) {
          prog_wait(f_oth, seen, st < 9 ? st + 2 : 0);
          STAMPL(2 + st);
          SB();
        }
        if (sec < 2) acc[uu][0] = mfma(wf[u], xf[ks][0], acc[uu][0]); else acc[uu][0] = mfma(xf[ks][0], wf[u], acc[uu][0]);
        SB();
        const bf16x4 lo = lds_tr4(A_ADR(tu, 0));
        SB();
        if (sec < 2) acc[uu][1] = mfma(wf[u], xf[ks][1], acc[uu][1]); else acc[uu][1] = mfma(xf[ks][1], wf[u], acc[uu][1]);
        SB();
        const bf16x4 hi = lds_tr4(A_ADR(tu, 1));
        if (u < 24) wf[u + 6] = cat8(lo, hi); else wn[u - 24] = cat8(lo, hi);
        if (u >= 19 && u < 24) { wc_[u - 19] = wa[u - 19] + sbn; OPAQUE(wc_[u - 19]); }
        if (u == 12) { xc_[0] = xa[0] + sbn; OPAQUE(xc_[0]); }
        if (u == 13) { hc_[0] = xh[0] + sbn; OPAQUE(hc_[0]); }
        if (u == 16) { xc_[1] = xa[1] + sbn; OPAQUE(xc_[1]); }
        if (u == 17) { hc_[1] = xh[1] + sbn; OPAQUE(hc_[1]); }
        if (u == 23) prog_set(f_own, st + 1);
        SB();
        if (uu == 5) hk0 = mfma(wf[u], xf[ks][2], hk0);
        if (uu == 9) hkx = mfma(wf[u], xf[ks][2], hkx);
        if (uu == 10) hv0 = mfma(xf[ks][2], wf[u], hv0);
        if (uu == 14) hvx = mfma(xf[ks][2], wf[u], hvx);
        SB();
        if (u == 5) xf[1][0] = lds_b128(xc_[1]);
        if (u == 9) xf[1][1] = lds_b128(xc_[1] + 2048u);
        if (u == 10) xf[1][2] = lds_b128(hc_[1]);
        if (u == 20) seen = prog_read(f_oth);
        if (u == 24) xn[0] = lds_b128(xc_[0]);
        if (u == 25) xn[1] = lds_b128(xc_[0] + 2048u);
        if (u == 29) { xn[2] = lds_b128(hc_[0]); sbn = sbn == 2 * A_STAGE ? 0u : sbn + A_STAGE; }
        SB();
      }
    }
    STAMP(12);
    const unsigned ex0 = lds0 + 2 * A_STAGE + lane * 16u;
    u32x2 Kp[5][2], Vp[5][2];
#pragma unroll
    for (int i = 0; i < 5; ++i)
#pragma unroll
      for (int bk = 0; bk < 2; ++bk) { Kp[i][bk] = pk4(acc[5 + i][bk]); Vp[i][bk] = pk4(acc[10 + i][bk]); }
    prog_wait(f_oth + 1024u, prog_read(f_oth + 1024u), 9);
    {
      if (w < 3) {
#pragma unroll
        for (int i = 0; i < 5; ++i) {
          unsigned slot = i < 4 ? ((i + w) & 3) : 4;
          u32x4 v = {Kp[i][1][0], Kp[i][1][1], Vp[i][1][0], Vp[i][1][1]};
          *(LAS u32x4*)(size_t)(ex0 + (w + 1) * 5120u + slot * 1024u) = v;
        }
      }
      u32x2 a = pk4(hk0), bb = pk4(hv0);
      u32x4 v = {a[0], a[1], bb[0], bb[1]};
      *(LAS u32x4*)(size_t)(ex0 + w * 1024u) = v;
      if (w == 0) {
        u32x2 a2 = pk4(hkx), b2 = pk4(hvx);
        u32x4 v2 = {a2[0], a2[1], b2[0], b2[1]};
        *(LAS u32x4*)(size_t)(ex0 + 4 * 1024u) = v2;
      }
    }
    const int par = li & 1;
    const bool cura = (4 * g + par) <= li, curb = (4 * g + par + 2) <= li;
    const unsigned hm = par ? 0xffff0000u : 0x0000ffffu;
    const unsigned ma0 = cura ? 0u : hm, ma1 = cura ? hm : 0u, mb0 = curb ? 0u : hm, mb1 = curb ? hm : 0u;
    const float SC = 0.22360679774997896f * 1.4426950408889634f, NEG = -1e9f * 1.4426950408889634f;
    const bool first = (rt == 0) && (w == 0);
    const unsigned vz = lane * 16u;
    u32x2 Kh[5], Vh[5];
    auto attend = [&](const int bk) {
      const bool fm = first && bk == 0;
      const float sca = (fm && !cura) ? 0.f : SC, bia = (fm && !cura) ? NEG : 0.f;
      const float scb = (fm && !curb) ? 0.f : SC, bib = (fm && !curb) ? NEG : 0.f;
      f32x4 ox = {};
      const int tb = rt * 8 + 2 * w + bk;
      const u32x2 Qx = pk4(acc[4][bk]);
#pragma unroll
      for (int i = 0; i < 4; ++i) {
        const int a = (i + w) & 3;
        const unsigned mq = (g == a) ? 0xffffffffu : 0u, mv = (q == a) ? 0xffffffffu : 0u;
        const u32x2 Qm = pk4(acc[i][bk]);
        const u32x4 Qf = {Qm[0], Qm[1], Qx[0] & mq, Qx[1] & mq};
        const u32x2 kp = bk == 0 ? Kh[i] : Kp[i][0], kxp = bk == 0 ? Kh[4] : Kp[4][0];
        const u32x4 Kprev = {kp[0], kp[1], kxp[0], kxp[1]};
        const u32x4 Kcur = {Kp[i][bk][0], Kp[i][bk][1], Kp[4][bk][0], Kp[4][bk][1]};
        f32x4 s0 = mfmau(Kprev, Qf, f32x4{}), s1 = mfmau(Kcur, Qf, f32x4{});
        float s0a = par ? s0[1] : s0[0], s0b = par ? s0[3] : s0[2];
        float s1a = par ? s1[1] : s1[0], s1b = par ? s1[3] : s1[2];
        float la = (cura ? s1a : s0a) * sca + bia, lb = (curb ? s1b : s0b) * scb + bib;
        float m = xg_max(vmax(la, lb));
        float ea = __builtin_amdgcn_exp2f(la - m), eb = __builtin_amdgcn_exp2f(lb - m);
        float inv = __builtin_amdgcn_rcpf(xg_sum(ea + eb));
        unsigned ta = pk2(ea * inv, ea * inv), tbb = pk2(eb * inv, eb * inv);
        const u32x4 P = {ta & ma0, tbb & mb0, ta & ma1, tbb & mb1};
        const u32x2 vprev = bk == 0 ? Vh[i] : Vp[i][0], vxp = bk == 0 ? Vh[4] : Vp[4][0];
        f32x4 om = mfmau(u32x4{vprev[0], vprev[1], Vp[i][bk][0], Vp[i][bk][1]}, P, f32x4{});
        ox = mfmau(u32x4{vxp[0] & mv, vxp[1] & mv, Vp[4][bk][0] & mv, Vp[4][bk][1] & mv}, P, ox);
        *(u32x2*)(zb + (size_t)((tb * KS2 + 2 * G + (a >> 1)) * 1024 + 8 * (a & 1)) + (size_t)vz) = pk4(om);
      }
      *(u32x2*)(zb + (size_t)((tb * KS2 + 16 + (G >> 1)) * 1024 + 8 * (G & 1)) + (size_t)vz) = pk4(ox);
    };
    attend(1);
    LGKM_BAR();
#pragma unroll
    for (int i = 0; i < 5; ++i) {
      unsigned slot = i < 4 ? ((i + w) & 3) : 4;
      u32x4 v = *(LAS u32x4*)(size_t)(ex0 + w * 5120u + slot * 1024u);
      Kh[i] = u32x2{v[0], v[1]}; Vh[i] = u32x2{v[2], v[3]};
    }
    attend(0);
  } else {
    const int wr = w >> 1, wc = w & 1;
    const unsigned f = 2 * (g & 1) + (q >> 1);
    unsigned wa[6];
#pragma unroll
    for (int n = 0; n < 6; ++n) wa[n] = lds0 + lrow * 384u + 32u * ((6 * wc + n) ^ f) + 8u * pq;
    unsigned xa[2];
#pragma unroll
    for (int ks = 0; ks < 2; ++ks) xa[ks] = lds0 + M_WB + (64 * wr + li) * 128u + 16u * ((4 * ks + g) ^ (lane & 7));
    f32x4 acc[6][4];
    {
      const int L = ((idx - 16) * 4 + w) * 36 + (lane < 36 ? lane : 0);
      const char* pa; unsigned stride;
      if (L < 528) { int r = (xcd >> 1) * 512 - 16 + L; r = r < 0 ? 0 : r; pa = (const char*)p.x + r * 1280; stride = 128u; }
      else if (L < 1488) { const int t = L - 528, k = t / 15, j = t % 15; pa = (const char*)p.wqkv + k * 3840 + (j / 5) * 1280 + ch * 640 + (j % 5) * 128; stride = 245760u; }
      else { const int t = (L < 2256 ? L : 1488) - 1488, k = t / 12, j = t % 12; pa = (const char*)p.w1 + k * 3072 + ch * 1536 + j * 128; stride = 196608u; }
      pa += 3 * (size_t)stride;
#pragma unroll
      for (int st = 3; st < 10; ++st) { touch(pa, lds0 + K1_FLAGS + 2048u); pa += stride; }
    }
    bf16x8 wn[3], xn[4];
    unsigned wc_[6], xc_[2];
#define M_RDW(u) lds_tr8(wc_[(u) % 6] + (32 * ((u) / 6)) * 384u, wc_[(u) % 6] + (32 * ((u) / 6) + 8) * 384u)
#define M_SETADDR(sb) do { _Pragma("unroll") for (int i = 0; i < 6; ++i) { wc_[i] = wa[i] + (sb); OPAQUE(wc_[i]); } \
      _Pragma("unroll") for (int i = 0; i < 2; ++i) { xc_[i] = xa[i] + (sb); OPAQUE(xc_[i]); } } while (0)
    M_SETADDR(0u);
    LGKM_BAR();
    prog_wait_sleep(f_oth, 1);
    STAMP(1);
    wn[0] = M_RDW(0);
#pragma unroll
    for (int i = 0; i < 4; ++i) xn[i] = lds_b128(xc_[0] + 2048u * i);
    wn[1] = M_RDW(1); wn[2] = M_RDW(2);
#pragma unroll
    for (int n = 0; n < 6; ++n) {
      f32x4 bi = bf4_to_f32(*(LAS u32x2*)(size_t)(lds0 + M_BIAS + (96 * wc + 16 * n + 4 * g) * 2));
#pragma unroll
      for (int m = 0; m < 4; ++m) acc[n][m] = bi;
    }
    SB();
    unsigned sbn = M_STAGE;
#define M_ADR(u, t) (wc_[(u) % 6] + (32 * ((u) / 6) + 8 * (t)) * 384u)
    for (int st = 0; st < 10; ++st) {
      bf16x8 wf[12], xf[2][4];
      unsigned seen = 0;
#pragma unroll
      for (int i = 0; i < 3; ++i) wf[i] = wn[i];
#pragma unroll
      for (int i = 0; i < 4; ++i) xf[0][i] = xn[i];
#pragma unroll
      for (int u = 0; u < 12; ++u) {
        const int ks = u / 6, n = u % 6, tu = (u + 3) % 12;
        if (u == 8) {
          prog_wait(f_oth, seen, st < 9 ? st + 2 : 0);
          STAMPL(2 + st);
          SB();
        }
        acc[n][0] = mfma(wf[u], xf[ks][0], acc[n][0]);
        SB();
        const bf16x4 lo = lds_tr4(M_ADR(tu, 0));
        SB();
        acc[n][1] = mfma(wf[u], xf[ks][1], acc[n][1]);
        SB();
        const bf16x4 hi = lds_tr4(M_ADR(tu, 1));
        if (u < 9) wf[u + 3] = cat8(lo, hi); else wn[u - 9] = cat8(lo, hi);
        SB();
        acc[n][2] = mfma(wf[u], xf[ks][2], acc[n][2]);
        SB();
        if (u < 4) xf[1][u] = lds_b128(xc_[1] + 2048u * u);
        if (u == 5) seen = prog_read(f_oth);
        if (u >= 8) xn[u - 8] = lds_b128(xc_[0] + 2048u * (u - 8));
        SB();
        acc[n][3] = mfma(wf[u], xf[ks][3], acc[n][3]);
        SB();
        if (u >= 3 && u < 9) { wc_[u - 3] = wa[u - 3] + sbn; OPAQUE(wc_[u - 3]); }
        if (u == 1) { xc_[0] = xa[0] + sbn; OPAQUE(xc_[0]); }
        if (u == 9) { xc_[1] = xa[1] + sbn; OPAQUE(xc_[1]); }
        if (u == 8) prog_set(f_own, st + 1);
        if (u == 11) sbn = sbn == 2 * M_STAGE ? 0u : sbn + M_STAGE;
        SB();
      }
    }
    STAMP(12);
    const float C1 = -2.f * 0.7978845608028654f * 1.4426950408889634f, C2 = C1 * 0.044715f;
    const unsigned vz = lane * 16u;
#pragma unroll
    for (int m = 0; m < 4; ++m) {
      const int tb = rt * 8 + 4 * wr + m;
#pragma unroll
      for (int j = 0; j < 3; ++j) {
        u32x4 o;
#pragma unroll
        for (int e = 0; e < 2; ++e) {
#pragma unroll
          for (int k = 0; k < 2; ++k) {
            f32x2 x = {acc[2 * j + e][m][2 * k], acc[2 * j + e][m][2 * k + 1]};
            f32x2 t = x * (x * x * C2 + C1);
            f32x2 d = {__builtin_amdgcn_exp2f(t[0]), __builtin_amdgcn_exp2f(t[1])};
            d = d + 1.f;
            f32x2 r = {__builtin_amdgcn_rcpf(d[0]), __builtin_amdgcn_rcpf(d[1])};
            r = r * x;
            o[2 * e + k] = pk2(r[0], r[1]);
          }
        }
        *(u32x4*)(zb + (size_t)((tb * KS2 + 20 + 6 * G + 3 * wc + j) * 1024) + (size_t)vz) = o;
      }
    }
  }
  STAMP(13);
  STAMP_OUT(0);
}

constexpr unsigned K2_STAGE = 20480, K2_NB = 7, K2_FLAGS = K2_STAGE * K2_NB, K2_GB = K2_FLAGS + 2048 + 256, K2_LDS = K2_GB + 2048;
struct K2Args { const void* z; const void* wo; const void* w2; const void* b2; const float* gate; void* out; };

__global__ __launch_bounds__(512, 2) void k2(K2Args p) {
  extern __shared__ __attribute__((aligned(16))) char shm[];
  const unsigned lds0 = (unsigned)(uintptr_t)(LAS char*)shm;
  const int lane = threadIdx.x & 63;
  const int wid = __builtin_amdgcn_readfirstlane(threadIdx.x >> 6);
  const int b = blockIdx.x, xcd = b & 7, idx = b >> 3;
  const int rq = xcd >> 1, ch = xcd & 1;
  const int rti = idx % 6, cn = 5 * ch + idx / 6;
  const int tb0 = rq * 32 + rti * 6;
  const int nblk = rti < 5 ? 6 : 2;
  const unsigned f_own = lds0 + K2_FLAGS + wid * 256u + lane * 4u;
  const unsigned f_oth = lds0 + K2_FLAGS + (wid < 4 ? 1024u : 0u) + (lane & 3) * 260u;
  prog_set(f_own, 0u);
  STAMP_DECL
  STAMP(0); STAMP_RT(61);

  if (wid < 4) {
    const int lw = wid;
    const srd_t sz = make_srd(p.z, Z_BYTES);
    const srd_t so = make_srd(p.wo, D_ * D_ * 2);
    const srd_t s2 = make_srd(p.w2, F_ * D_ * 2);
    const unsigned rho = 8 * lw + (lane >> 3), t = rho >> 4, gg = (rho >> 2) & 3, qq = rho & 3;
    const unsigned cpos = lane & 7, sch = (((cpos >> 1) ^ ((rho >> 1) & 3)) << 1) | (cpos & 1);
    const unsigned cb = 128u * cn + 16u * sch;
    const unsigned vA = (20 * t + (rho & 15)) * 1280u + cb;
    const unsigned vB = (80 * t + 20 * gg + 16 + qq) * 1280u + cb;
    const unsigned vC = rho * 1280u + cb;
    const unsigned vz = lane * 16u;
    unsigned zo[3];
#pragma unroll
    for (int i = 0; i < 3; ++i) { int n = 3 * lw + i, blk = n >> 1; blk = blk < nblk ? blk : 0; zo[i] = (unsigned)(((tb0 + blk) * KS2 + (n & 1)) * 1024); }
    const srd_t sq = make_srd(g_seq, 256);
    const unsigned vzero = 0, f_row = lds0 + K2_FLAGS + wid * 256u;
    auto issue = [&](int st, unsigned lb, srd_t sw, unsigned vw, unsigned wo0, unsigned wstep) {
#pragma unroll
      for (int i = 0; i < 3; ++i) dma16(vz, sz, zo[i] + 2048u * st, lb + 1024u * (3 * lw + i));
      dma16(vw, sw, wo0, lb + 12288u + 1024u * lw);
      dma16(vw, sw, wo0 + wstep, lb + 16384u + 1024u * lw);
    };
    STAMP(1);
    issue(0, lds0, so, vA, 0u, 51200u);
    if (lw == 0) dma16(vz, make_srd(p.gate, S_ * 4), tb0 * 64u, lds0 + K2_GB);
    if (lw == 1) dma16(vz, make_srd(p.b2, D_ * 2), cn * 128u, lds0 + K2_GB + 1024u);
    LGKM_BAR();
    dma4(vzero, sq, 4, f_row);
#pragma unroll
    for (int st = 1; st < 7; ++st) { issue(st, lds0 + st * K2_STAGE, so, vA, 102400u * st, 51200u); dma4(vzero, sq, 4 * st + 4, f_row); }
    prog_wait_sleep(f_oth, 1);
    issue(7, lds0, so, vA, 102400u * 7, 51200u); dma4(vzero, sq, 32, f_row);
    prog_wait_sleep(f_oth, 2);
    issue(8, lds0 + K2_STAGE, so, vB, 0u, 204800u); dma4(vzero, sq, 36, f_row);
    prog_wait_sleep(f_oth, 3);
    issue(9, lds0 + 2 * K2_STAGE, so, vB, 409600u, 204800u); dma4(vzero, sq, 40, f_row);
    unsigned lb = lds0 + 3 * K2_STAGE;
    unsigned seen = prog_read(f_oth);
    for (int st = 10; st < 34; ++st) {
      if (__builtin_amdgcn_ballot_w64(seen < (unsigned)(st - (K2_NB - 1))) != 0) prog_wait_sleep(f_oth, st - (K2_NB - 1));
      seen = prog_read(f_oth);
      ISS(issue(SST(st), lb, s2, vC, 81920u * SST(st - 10), 40960u));
      dma4(vzero, sq, 4 * st + 4, f_row);
      STAMPL(st);
      lb = lb == lds0 + (K2_NB - 1) * K2_STAGE ? lds0 : lb + K2_STAGE;
    }
    STAMP(40);
    STAMP_OUT(1);
    return;
  }
  __builtin_amdgcn_s_setprio(2);
  const int w = wid - 4, wr = w >> 1, wc = w & 1;
  const int g = lane >> 4, q = (lane & 15) >> 2, pq = lane & 3, li = lane & 15;
  const unsigned za = lds0 + wr * 3 * 2048u + lane * 16u;
  unsigned wa[2];
  {
    const unsigned r = 4 * g + q;
#pragma unroll
    for (int n = 0; n < 2; ++n) wa[n] = lds0 + 12288u + r * 128u + 32u * ((2 * wc + n) ^ ((r >> 1) & 3)) + 8u * pq;
  }
  f32x4 acc[2][2][3];
#pragma unroll
  for (int h = 0; h < 2; ++h)
#pragma unroll
    for (int n = 0; n < 2; ++n)
#pragma unroll
      for (int m = 0; m < 3; ++m) acc[h][n][m] = f32x4{};
  bf16x8 zf[2][2][3], wf[2][2][2];
  unsigned zc = za, wc0 = wa[0], wc1 = wa[1], sbn = K2_STAGE;
  LGKM_BAR();
  prog_wait_sleep(f_oth, 1);
  STAMP(1);
  unsigned seen = prog_read(f_oth);
  SB();
#pragma unroll
  for (int e = 0; e < 2; ++e) {
#pragma unroll
    for (int m = 0; m < 3; ++m) zf[0][e][m] = lds_b128(zc + 2048u * m + 1024u * e);
    wf[0][e][0] = lds_tr8(wc0 + 4096u * e, wc0 + 4096u * e + 2048u);
    wf[0][e][1] = lds_tr8(wc1 + 4096u * e, wc1 + 4096u * e + 2048u);
  }
  prog_set(f_own, 1);
  auto stage = [&](const int st, const int pp, f32x4 (&a)[2][3]) {
    prog_wait(f_oth, seen, st < 33 ? st + 2 : 0);
    STAMPL(2 + st);
    SB();
    bf16x4 lo, hi;
#pragma unroll
    for (int e = 0; e < 2; ++e) {
      const unsigned eo = 4096u * e;
      MF(a[0][0], wf[pp][e][0], zf[pp][e][0]); SB();
      lo = lds_tr4(wc0 + eo); hi = lds_tr4(wc0 + eo + 2048u); wf[pp ^ 1][e][0] = cat8(lo, hi); SB();
      MF(a[0][1], wf[pp][e][0], zf[pp][e][1]); SB();
      zf[pp ^ 1][e][0] = lds_b128(zc + 1024u * e); SB();
      MF(a[0][2], wf[pp][e][0], zf[pp][e][2]); SB();
      zf[pp ^ 1][e][1] = lds_b128(zc + 2048u + 1024u * e); SB();
      MF(a[1][0], wf[pp][e][1], zf[pp][e][0]); SB();
      zf[pp ^ 1][e][2] = lds_b128(zc + 4096u + 1024u * e); SB();
      MF(a[1][1], wf[pp][e][1], zf[pp][e][1]); SB();
      lo = lds_tr4(wc1 + eo); hi = lds_tr4(wc1 + eo + 2048u); wf[pp ^ 1][e][1] = cat8(lo, hi); SB();
      MF(a[1][2], wf[pp][e][1], zf[pp][e][2]); SB();
      if (e == 0) seen = prog_read(f_oth);
      else {
        prog_set(f_own, st + 2);
        zc = za + sbn; wc0 = wa[0] + sbn; wc1 = wa[1] + sbn;
        OPAQUE(zc); OPAQUE(wc0); OPAQUE(wc1);
        sbn = sbn == (K2_NB - 1) * K2_STAGE ? 0u : sbn + K2_STAGE;
      }
      SB();
    }
  };
  zc = za + K2_STAGE; wc0 = wa[0] + K2_STAGE; wc1 = wa[1] + K2_STAGE; sbn = 2 * K2_STAGE;
  for (int st = 0; st < 10; st += 2) { stage(st, 0, acc[0]); stage(st + 1, 1, acc[0]); }
  for (int st = 10; st < 34; st += 2) { stage(st, 0, acc[1]); stage(st + 1, 1, acc[1]); }
  asm volatile("s_nop 7\n\ts_nop 7" ::: "memory");
  SB();
  STAMP(40);
  float gt[3];
  u32x2 bv[2];
#pragma unroll
  for (int m = 0; m < 3; ++m) gt[m] = *(LAS float*)(size_t)(lds0 + K2_GB + ((3 * wr + m) * 16 + li) * 4);
#pragma unroll
  for (int n = 0; n < 2; ++n) bv[n] = *(LAS u32x2*)(size_t)(lds0 + K2_GB + 1024u + (32 * wc + 16 * n + 4 * g) * 2);
#pragma unroll
  for (int m = 0; m < 3; ++m) {
    const int blk = 3 * wr + m;
    if (blk < nblk) {
      const int tok = (tb0 + blk) * 16 + li;
#pragma unroll
      for (int n = 0; n < 2; ++n) {
        const int col = 64 * cn + 32 * wc + 16 * n + 4 * g;
        f32x4 r = acc[0][n][m] * gt[m] + (acc[1][n][m] + bf4_to_f32(bv[n])) * (1.f - gt[m]);
        *(u32x2*)((char*)p.out + ((size_t)tok * D_ + col) * 2) = pk4(r);
      }
    }
  }
  STAMP(41);
  STAMP_OUT(1);
}

extern "C" void kernel_launch(void* const* d_in, const int* in_sizes, int n_in, void* d_out, int out_size, void* d_ws,
                              size_t ws_size, hipStream_t stream) {
  (void)in_sizes; (void)n_in; (void)out_size; (void)ws_size;
  (void)hipFuncSetAttribute((const void*)k1, hipFuncAttributeMaxDynamicSharedMemorySize, K1_LDS);
  (void)hipFuncSetAttribute((const void*)k2, hipFuncAttributeMaxDynamicSharedMemorySize, K2_LDS);
  char* ws = (char*)d_ws;
  K1Args a1{d_in[0], d_in[1], d_in[4], d_in[5], d_in[8], ws, (float*)(ws + Z_BYTES), d_in[2], d_in[6], d_out, d_in[7]};
  K2Args a2{ws, d_in[2], d_in[6], d_in[7], (const float*)(ws + Z_BYTES), d_out};
  k1<<<256, 512, K1_LDS, stream>>>(a1);
  k2<<<240, 512, K2_LDS, stream>>>(a2);
}
```

```cpp
#define AMPX 1
#define BA 0
#define WA 4
#define SA 0
#define KB 0
#define BB 0
#define WB 4
#define SB_ 0
#include <hip/hip_runtime.h>
#include <hip/hip_bf16.h>
#include <stdint.h>

typedef __attribute__((ext_vector_type(8))) __bf16 bf16x8;
typedef __attribute__((ext_vector_type(4))) __bf16 bf16x4;
typedef __attribute__((ext_vector_type(2))) __bf16 bf16x2;
typedef __attribute__((ext_vector_type(4))) float f32x4;
typedef __attribute__((ext_vector_type(2))) float f32x2;
typedef __attribute__((ext_vector_type(4))) unsigned u32x4;
typedef __attribute__((ext_vector_type(2))) unsigned u32x2;
typedef __attribute__((ext_vector_type(8))) unsigned u32x8;
typedef __amdgpu_buffer_rsrc_t srd_t;
#define LAS __attribute__((address_space(3)))
#define DI __device__ __forceinline__

constexpr int S_ = 2048, D_ = 640, F_ = 1536;
constexpr int KS2 = 68;
constexpr unsigned Z_BYTES = 128u * KS2 * 1024u;

DI srd_t make_srd(const void* p, unsigned bytes) {
  return __builtin_amdgcn_make_buffer_rsrc((void*)p, (short)0, (int)bytes, 0x00020000);
}
DI void dma16(unsigned voff, srd_t srd, unsigned soff, unsigned lds) {
  asm volatile("s_mov_b32 m0, %0\n\ts_nop 0\n\tbuffer_load_dwordx4 %1, %2, %3 offen lds"
               :: "s"(lds), "v"(voff), "s"(srd), "s"(soff) : "memory");
}
__device__ const unsigned g_seq[64] = {0,  1,  2,  3,  4,  5,  6,  7,  8,  9,  10, 11, 12, 13, 14, 15, 16, 17, 18, 19, 20, 21,
                                       22, 23, 24, 25, 26, 27, 28, 29, 30, 31, 32, 33, 34, 35, 36, 37, 38, 39, 40, 41, 42, 43,
                                       44, 45, 46, 47, 48, 49, 50, 51, 52, 53, 54, 55, 56, 57, 58, 59, 60, 61, 62, 63};
DI void dma4(unsigned voff, srd_t srd, unsigned soff, unsigned lds) {
  asm volatile("s_mov_b32 m0, %0\n\ts_nop 0\n\tbuffer_load_dword %1, %2, %3 offen lds"
               :: "s"(lds), "v"(voff), "s"(srd), "s"(soff) : "memory");
}
DI void touch(const char* addr, unsigned lds_scratch) {
  asm volatile("s_mov_b32 m0, %0\n\ts_nop 0\n\tglobal_load_lds_dword %1, off" :: "s"(lds_scratch), "v"(addr) : "memory");
}
template <unsigned HI> DI void dma16m(unsigned voff, srd_t srd, unsigned soff, unsigned lds) {
  asm volatile("s_mov_b32 m0, %0\n\ts_mov_b32 exec_hi, %4\n\tbuffer_load_dwordx4 %1, %2, %3 offen lds\n\ts_mov_b32 exec_hi, -1"
               :: "s"(lds), "v"(voff), "s"(srd), "s"(soff), "i"(HI) : "memory");
}
DI bf16x8 lds_tr8(unsigned a0, unsigned a1) {
  bf16x4 lo = __builtin_amdgcn_ds_read_tr16_b64_v4bf16((LAS bf16x4*)(size_t)a0);
  bf16x4 hi = __builtin_amdgcn_ds_read_tr16_b64_v4bf16((LAS bf16x4*)(size_t)a1);
  return __builtin_shufflevector(lo, hi, 0, 1, 2, 3, 4, 5, 6, 7);
}
DI bf16x4 lds_tr4(unsigned a) { return __builtin_amdgcn_ds_read_tr16_b64_v4bf16((LAS bf16x4*)(size_t)a); }
DI bf16x8 cat8(bf16x4 lo, bf16x4 hi) { return __builtin_shufflevector(lo, hi, 0, 1, 2, 3, 4, 5, 6, 7); }
DI bf16x8 lds_b128(unsigned a) { return *(LAS bf16x8*)(size_t)a; }
DI f32x4 mfma(bf16x8 a, bf16x8 b, f32x4 c) { return __builtin_amdgcn_mfma_f32_16x16x32_bf16(a, b, c, 0, 0, 0); }
DI f32x4 mfmau(u32x4 a, u32x4 b, f32x4 c) { return mfma(__builtin_bit_cast(bf16x8, a), __builtin_bit_cast(bf16x8, b), c); }
DI unsigned pk2(float a, float b) { bf16x2 v = {(__bf16)a, (__bf16)b}; return __builtin_bit_cast(unsigned, v); }
DI u32x2 pk4(f32x4 v) { u32x2 r = {pk2(v[0], v[1]), pk2(v[2], v[3])}; return r; }
DI float vmax(float a, float b) { return __builtin_amdgcn_fmed3f(a, b, __builtin_inff()); }
DI float xg_max(float x) {
  auto s = __builtin_amdgcn_permlane16_swap(__float_as_uint(x), __float_as_uint(x), false, false);
  x = vmax(__uint_as_float(s[0]), __uint_as_float(s[1]));
  auto t = __builtin_amdgcn_permlane32_swap(__float_as_uint(x), __float_as_uint(x), false, false);
  return vmax(__uint_as_float(t[0]), __uint_as_float(t[1]));
}
DI float xg_sum(float x) {
  auto s = __builtin_amdgcn_permlane16_swap(__float_as_uint(x), __float_as_uint(x), false, false);
  x = __uint_as_float(s[0]) + __uint_as_float(s[1]);
  auto t = __builtin_amdgcn_permlane32_swap(__float_as_uint(x), __float_as_uint(x), false, false);
  return __uint_as_float(t[0]) + __uint_as_float(t[1]);
}
DI f32x4 bf4_to_f32(u32x2 v) {
  return f32x4{__uint_as_float(v[0] << 16), __uint_as_float(v[0] & 0xffff0000u), __uint_as_float(v[1] << 16), __uint_as_float(v[1] & 0xffff0000u)};
}
#define STAMP_DECL
#define STAMP(slot)
#define STAMPL(slot)
#define STAMP_RT(slot)
#define STAMP_OUT(kid)
#define SST(x) (x)
#define ISS(x) x
#define MF(c, a, b) asm volatile("v_mfma_f32_16x16x32_bf16 %0, %1, %2, %0" : "+v"(c) : "v"(a), "v"(b))
#undef STAMP_DECL
#undef STAMP
#undef STAMP_OUT
#define STAMP_DECL unsigned long long amp_a = 0, amp_b = 0;
#define STAMP(slot) do{ if ((slot) == SA || (slot) == SB_) { unsigned long long _c; asm volatile("s_memrealtime %0\n\ts_waitcnt lgkmcnt(0)" : "=s"(_c) :: "memory"); if ((slot) == SA) amp_a = _c; if ((slot) == SB_) amp_b = _c; } }while(0)
#define STAMP_OUT(kid) do{ asm volatile("s_waitcnt vmcnt(0)":::"memory"); STAMP(62); \
   if ((kid) == 0 && blockIdx.x == BA && wid == WA && lane == 0) *(unsigned*)((char*)p.gate + 16384) = (unsigned)amp_a; \
   if ((kid) == 0 && KB == 0 && blockIdx.x == BB && wid == WB && lane == 0) *(unsigned*)((char*)p.gate + 16388) = (unsigned)amp_b; \
   if ((kid) == 1 && blockIdx.x == (KB == 1 ? BB : 0) && wid == 4) { unsigned a = __builtin_amdgcn_readfirstlane(*(const unsigned*)((const char*)p.gate + 16384)); \
      unsigned bb = KB == 1 ? (unsigned)amp_b : __builtin_amdgcn_readfirstlane(*(const unsigned*)((const char*)p.gate + 16388)); unsigned d = bb - a + 100u; \
      unsigned long long t0 = __builtin_amdgcn_s_memrealtime(); d = d > 4000u ? 4000u : d; while (__builtin_amdgcn_s_memrealtime() - t0 < 10ull * d) {} } }while(0)
#define SB() __builtin_amdgcn_sched_barrier(0)
#define OPAQUE(x) asm volatile("" : "+v"(x))
#define WAITV(N) asm volatile("s_waitcnt vmcnt(" #N ")" ::: "memory")
#define LGKM_BAR() asm volatile("s_waitcnt lgkmcnt(0)\n\ts_barrier" ::: "memory")
#define FENCE() asm volatile("" ::: "memory")

DI void prog_set(unsigned row_addr  , unsigned v) {
  FENCE();
  __hip_atomic_store((LAS unsigned*)(size_t)row_addr, v, __ATOMIC_RELAXED, __HIP_MEMORY_SCOPE_WORKGROUP);
  FENCE();
}
DI unsigned prog_read(unsigned addr  ) { return __hip_atomic_load((LAS unsigned*)(size_t)addr, __ATOMIC_RELAXED, __HIP_MEMORY_SCOPE_WORKGROUP); }
DI void prog_wait(unsigned addr, unsigned seen, unsigned need) {
  if (__builtin_expect(__builtin_amdgcn_ballot_w64(seen < need) != 0, 0)) {
    unsigned long long m;
    do {
      unsigned v;
      asm volatile("ds_read_b32 %0, %1\n\ts_waitcnt lgkmcnt(0)" : "=v"(v) : "v"(addr) : "memory");
      m = __builtin_amdgcn_ballot_w64(v < need);
    } while (m != 0);
  }
  FENCE();
}
DI void prog_wait_sleep(unsigned addr, unsigned need) {
  while (__builtin_amdgcn_ballot_w64(prog_read(addr) < need) != 0) __builtin_amdgcn_s_sleep(1);
  FENCE();
}

DI unsigned swz23(unsigned k) { return (k & ~12u) | ((k & 4u) << 1) | ((k & 8u) >> 1); }

constexpr unsigned A_WB = 30720, A_STAGE = 49152;
constexpr unsigned M_WB = 24576, M_STAGE = 40960;
constexpr unsigned M_BIAS = 3 * M_STAGE;
constexpr unsigned K1_FLAGS = 3 * A_STAGE, K1_LDS = K1_FLAGS + 2048 + 256;

struct K1Args { const void* x; const void* wqkv; const void* w1; const void* b1; const void* gw; void* z; float* gate; const void* wo; const void* w2; const void* out; const void* b2; };

__global__ __launch_bounds__(512, 2) void k1(K1Args p) {
  extern __shared__ __attribute__((aligned(16))) char shm[];
  const unsigned lds0 = (unsigned)(uintptr_t)(LAS char*)shm;
  const int lane = threadIdx.x & 63;
  const int wid = __builtin_amdgcn_readfirstlane(threadIdx.x >> 6);
  const int b = blockIdx.x, xcd = b & 7, idx = b >> 3;
  const int rt = (xcd >> 1) * 4 + (idx & 3), c = idx >> 2, ch = xcd & 1;
  const bool is_attn = c < 4;
  const int G = 4 * ch + (c & 3);
  const int T0 = rt * 128;
  const unsigned f_own = lds0 + K1_FLAGS + wid * 256u + lane * 4u;
  const unsigned f_oth = lds0 + K1_FLAGS + (wid < 4 ? 1024u : 0u) + (lane & 3) * 260u;
  prog_set(f_own, 0u);
  STAMP_DECL
  STAMP(0); STAMP_RT(61);

  if (wid < 4) {
    const int lw = wid;
    const srd_t sx = make_srd(p.x, S_ * D_ * 2);
    const srd_t sq = make_srd(g_seq, 256);
    const unsigned vzero = 0, f_row = lds0 + K1_FLAGS + wid * 256u;
    const unsigned vx = (lane >> 3) * 1280u + 16u * ((lane & 7) ^ (lane >> 3));
    if (is_attn) {
      const srd_t sw = make_srd(p.wqkv, D_ * 3 * D_ * 2);
      const unsigned r2 = lane >= 30 ? 1u : 0u, rem = lane - 30u * r2, sec = (rem >= 20 ? 2u : (rem >= 10 ? 1u : 0u));
      const unsigned vw = r2 * 3840u + sec * 1280u + 16u * (rem - 10u * sec);
      const int hr = 4 * lw + (lane >> 3);
      const int ht = T0 - 16 + hr;
      const unsigned vh = (unsigned)(ht < 0 ? 0 : ht) * 1280u + 16u * ((lane & 7) ^ (hr & 7));
      auto issueA = [&](int st, unsigned lb) {
        const unsigned sow = st * 245760u + 160u * G, sox = st * 128u + (unsigned)T0 * 1280u;
        dma16m<0u>(vh, sx, st * 128u, lb + A_WB + 512u * lw);
#pragma unroll
        for (int i = 0; i < 4; ++i) { const unsigned n = 4 * lw + i; dma16(vx, sx, sox + n * 10240u, lb + A_WB + 2048u + 1024u * n); }
#pragma unroll
        for (int i = 0; i < 2; ++i) { const unsigned n = 8 * lw + i; dma16m<0x0fffffffu>(vw, sw, sow + swz23(2 * n) * 3840u, lb + 960u * n); }
      };
      auto issueB = [&](int st, unsigned lb) {
        const unsigned sow = st * 245760u + 160u * G;
#pragma unroll
        for (int i = 2; i < 8; ++i) { const unsigned n = 8 * lw + i; dma16m<0x0fffffffu>(vw, sw, sow + swz23(2 * n) * 3840u, lb + 960u * n); }
      };
      STAMP(1);
      issueA(0, lds0); issueB(0, lds0);
      LGKM_BAR();
      dma4(vzero, sq, 4, f_row);
      issueA(1, lds0 + A_STAGE); issueB(1, lds0 + A_STAGE); dma4(vzero, sq, 8, f_row);
      issueA(2, lds0 + 2 * A_STAGE); issueB(2, lds0 + 2 * A_STAGE); dma4(vzero, sq, 12, f_row);
      unsigned lb = lds0;
      for (int st = 3; st < 10; ++st) {
        prog_wait_sleep(f_oth, st - 2);
        ISS(issueA(SST(st), lb));
        ISS(issueB(SST(st), lb));
        dma4(vzero, sq, 4 * st + 4, f_row);
        STAMPL(1 + st);
        lb = lb == lds0 + 2 * A_STAGE ? lds0 : lb + A_STAGE;
      }
    } else {
      const srd_t sw = make_srd(p.w1, D_ * F_ * 2);
      unsigned vw[3];
#pragma unroll
      for (int i = 0; i < 3; ++i) {
        const unsigned cw = 64 * i + lane, j = (cw * 171u) >> 12, cp = cw - 24u * j;
        vw[i] = (8u * (j >> 2) + (j & 3)) * 3072u + 32u * ((cp >> 1) ^ ((j >> 1) & 3)) + 16u * (cp & 1);
      }
      auto issueA = [&](int st, unsigned lb) {
        const unsigned sow = st * 196608u + 384u * G, sox = st * 128u + (unsigned)T0 * 1280u;
#pragma unroll
        for (int i = 0; i < 4; ++i) { const unsigned n = 4 * lw + i; dma16(vx, sx, sox + n * 10240u, lb + M_WB + 1024u * n); }
#pragma unroll
        for (int i = 0; i < 3; ++i) dma16(vw[i], sw, sow + (16u * (lw >> 1) + 4u * (lw & 1)) * 3072u, lb + 3072u * lw + 1024u * i);
      };
      auto issueB = [&](int st, unsigned lb) {
        const unsigned sow = st * 196608u + 384u * G;
#pragma unroll
        for (int i = 0; i < 3; ++i) dma16(vw[i], sw, sow + (32u + 16u * (lw >> 1) + 4u * (lw & 1)) * 3072u, lb + 3072u * (lw + 4) + 1024u * i);
      };
      STAMP(1);
      issueA(0, lds0); issueB(0, lds0);
      if (lw == 0) dma16(lane * 16u, make_srd(p.b1, F_ * 2), 384u * G, lds0 + M_BIAS);
      LGKM_BAR();
      dma4(vzero, sq, 4, f_row);
      issueA(1, lds0 + M_STAGE); issueB(1, lds0 + M_STAGE); dma4(vzero, sq, 8, f_row);
      issueA(2, lds0 + 2 * M_STAGE); issueB(2, lds0 + 2 * M_STAGE); dma4(vzero, sq, 12, f_row);
      unsigned lb = lds0;
      for (int st = 3; st < 10; ++st) {
        prog_wait_sleep(f_oth, st - 2);
        ISS(issueA(SST(st), lb));
        ISS(issueB(SST(st), lb));
        dma4(vzero, sq, 4 * st + 4, f_row);
        STAMPL(1 + st);
        lb = lb == lds0 + 2 * M_STAGE ? lds0 : lb + M_STAGE;
      }
#pragma unroll
      for (int j = 0; j < 3; ++j) {
        int L = (j * 64 + (idx - 16) * 4 + lw) * 64 + lane;
        L = L < 10880 ? L : 0;
        const int r = L / 5, cl = L % 5;
        const char* pa = (r < 640 ? (const char*)p.wo + r * 1280 : (const char*)p.w2 + (r - 640) * 1280) + ch * 640 + cl * 128;
        touch(pa, lds0 + K1_FLAGS + 2048u);
      }
    }
    STAMP(20);
    {
      const int cc = is_attn ? G : 8 + G;
      const int row = T0 + 8 * cc + 2 * lw + (lane >> 5);
      const int l5 = lane & 31;
      const u32x4* xr = (const u32x4*)((const char*)p.x + (size_t)row * 1280);
      const u32x4* gr = (const u32x4*)p.gw;
      float acc = 0.f;
#pragma unroll
      for (int j = 0; j < 3; ++j) {
        int ck = l5 + 32 * j;
        bool ok = ck < 80;
        int ckc = ok ? ck : 0;
        u32x4 xv = xr[ckc], gv = gr[ckc];
        float s = 0.f;
#pragma unroll
        for (int e = 0; e < 4; ++e) {
          s += __uint_as_float(xv[e] << 16) * __uint_as_float(gv[e] << 16);
          s += __uint_as_float(xv[e] & 0xffff0000u) * __uint_as_float(gv[e] & 0xffff0000u);
        }
        acc += ok ? s : 0.f;
      }
#pragma unroll
      for (int o = 16; o > 0; o >>= 1) acc += __shfl_xor(acc, o);
      if (l5 == 0) p.gate[row] = 1.f / (1.f + __expf(-acc));
    }
    STAMP_OUT(0);
    return;
  }

  __builtin_amdgcn_s_setprio(2);
  const int w = wid - 4;
  const int g = lane >> 4, q = (lane & 15) >> 2, pq = lane & 3, li = lane & 15;
  const unsigned lrow = 16 * (g >> 1) + 4 * (g & 1) + q;
  char* const zb = (char*)p.z;
  if (is_attn) {
    unsigned wa[5];
#pragma unroll
    for (int i = 0; i < 4; ++i) wa[i] = lds0 + lrow * 480u + 40u * ((i + w) & 3) + 8u * pq;
    wa[4] = lds0 + lrow * 480u + 40u * pq + 32u;
    unsigned xa[2], xh[2];
#pragma unroll
    for (int ks = 0; ks < 2; ++ks) {
      unsigned sw = 16u * ((4 * ks + g) ^ (lane & 7));
      xh[ks] = lds0 + A_WB + li * 128u + sw;
      xa[ks] = xh[ks] + 2048u * (1 + 2 * w);
    }
    f32x4 acc[15][2], hk0 = {}, hkx = {}, hv0 = {}, hvx = {};
#pragma unroll
    for (int u = 0; u < 15; ++u) { acc[u][0] = f32x4{}; acc[u][1] = f32x4{}; }

    bf16x8 wn[6], xn[3];
    unsigned wc_[5], xc_[2], hc_[2];
#define A_RDW(u) lds_tr8(wc_[(u) % 5] + (32 * ((u) / 15)) * 480u + (((u) % 15) / 5) * 160u, wc_[(u) % 5] + (32 * ((u) / 15) + 8) * 480u + (((u) % 15) / 5) * 160u)
#define A_SETADDR(sb) do { _Pragma("unroll") for (int i = 0; i < 5; ++i) { wc_[i] = wa[i] + (sb); OPAQUE(wc_[i]); } \
      _Pragma("unroll") for (int i = 0; i < 2; ++i) { xc_[i] = xa[i] + (sb); hc_[i] = xh[i] + (sb); OPAQUE(xc_[i]); OPAQUE(hc_[i]); } } while (0)
    A_SETADDR(0u);
    LGKM_BAR();
    prog_wait_sleep(f_oth, 1);
    STAMP(1);
    xn[0] = lds_b128(xc_[0]); xn[1] = lds_b128(xc_[0] + 2048u);
#pragma unroll
    for (int i = 0; i < 6; ++i) wn[i] = A_RDW(i);
    xn[2] = lds_b128(hc_[0]);
    SB();
    unsigned sbn = A_STAGE;
#define A_ADR(u, t) (wc_[(u) % 5] + (32 * ((u) / 15) + 8 * (t)) * 480u + (((u) % 15) / 5) * 160u)
    for (int st = 0; st < 10; ++st) {
      bf16x8 wf[30], xf[2][3];
      unsigned seen = 0;
#pragma unroll
      for (int i = 0; i < 6; ++i) wf[i] = wn[i];
#pragma unroll
      for (int i = 0; i < 3; ++i) xf[0][i] = xn[i];
#pragma unroll
      for (int u = 0; u < 30; ++u) {
        const int ks = u / 15, uu = u % 15, sec = uu / 5, tu = (u + 6) % 30;
        if (u == 24) {
          prog_wait(f_oth, seen, st < 9 ? st + 2 : 0);
          STAMPL(2 + st);
          SB();
        }
        if (sec < 2) acc[uu][0] = mfma(wf[u], xf[ks][0], acc[uu][0]); else acc[uu][0] = mfma(xf[ks][0], wf[u], acc[uu][0]);
        SB();
        const bf16x4 lo = lds_tr4(A_ADR(tu, 0));
        SB();
        if (sec < 2) acc[uu][1] = mfma(wf[u], xf[ks][1], acc[uu][1]); else acc[uu][1] = mfma(xf[ks][1], wf[u], acc[uu][1]);
        SB();
        const bf16x4 hi = lds_tr4(A_ADR(tu, 1));
        if (u < 24) wf[u + 6] = cat8(lo, hi); else wn[u - 24] = cat8(lo, hi);
        if (u >= 19 && u < 24) { wc_[u - 19] = wa[u - 19] + sbn; OPAQUE(wc_[u - 19]); }
        if (u == 12) { xc_[0] = xa[0] + sbn; OPAQUE(xc_[0]); }
        if (u == 13) { hc_[0] = xh[0] + sbn; OPAQUE(hc_[0]); }
        if (u == 16) { xc_[1] = xa[1] + sbn; OPAQUE(xc_[1]); }
        if (u == 17) { hc_[1] = xh[1] + sbn; OPAQUE(hc_[1]); }
        if (u == 23) prog_set(f_own, st + 1);
        SB();
        if (uu == 5) hk0 = mfma(wf[u], xf[ks][2], hk0);
        if (uu == 9) hkx = mfma(wf[u], xf[ks][2], hkx);
        if (uu == 10) hv0 = mfma(xf[ks][2], wf[u], hv0);
        if (uu == 14) hvx = mfma(xf[ks][2], wf[u], hvx);
        SB();
        if (u == 5) xf[1][0] = lds_b128(xc_[1]);
        if (u == 9) xf[1][1] = lds_b128(xc_[1] + 2048u);
        if (u == 10) xf[1][2] = lds_b128(hc_[1]);
        if (u == 20) seen = prog_read(f_oth);
        if (u == 24) xn[0] = lds_b128(xc_[0]);
        if (u == 25) xn[1] = lds_b128(xc_[0] + 2048u);
        if (u == 29) { xn[2] = lds_b128(hc_[0]); sbn = sbn == 2 * A_STAGE ? 0u : sbn + A_STAGE; }
        SB();
      }
    }
    STAMP(12);
    const unsigned ex0 = lds0 + 2 * A_STAGE + lane * 16u;
    u32x2 Kp[5][2], Vp[5][2];
#pragma unroll
    for (int i = 0; i < 5; ++i)
#pragma unroll
      for (int bk = 0; bk < 2; ++bk) { Kp[i][bk] = pk4(acc[5 + i][bk]); Vp[i][bk] = pk4(acc[10 + i][bk]); }
    prog_wait(f_oth + 1024u, prog_read(f_oth + 1024u), 9);
    {
      if (w < 3) {
#pragma unroll
        for (int i = 0; i < 5; ++i) {
          unsigned slot = i < 4 ? ((i + w) & 3) : 4;
          u32x4 v = {Kp[i][1][0], Kp[i][1][1], Vp[i][1][0], Vp[i][1][1]};
          *(LAS u32x4*)(size_t)(ex0 + (w + 1) * 5120u + slot * 1024u) = v;
        }
      }
      u32x2 a = pk4(hk0), bb = pk4(hv0);
      u32x4 v = {a[0], a[1], bb[0], bb[1]};
      *(LAS u32x4*)(size_t)(ex0 + w * 1024u) = v;
      if (w == 0) {
        u32x2 a2 = pk4(hkx), b2 = pk4(hvx);
        u32x4 v2 = {a2[0], a2[1], b2[0], b2[1]};
        *(LAS u32x4*)(size_t)(ex0 + 4 * 1024u) = v2;
      }
    }
    const int par = li & 1;
    const bool cura = (4 * g + par) <= li, curb = (4 * g + par + 2) <= li;
    const unsigned hm = par ? 0xffff0000u : 0x0000ffffu;
    const unsigned ma0 = cura ? 0u : hm, ma1 = cura ? hm : 0u, mb0 = curb ? 0u : hm, mb1 = curb ? hm : 0u;
    const float SC = 0.22360679774997896f * 1.4426950408889634f, NEG = -1e9f * 1.4426950408889634f;
    const bool first = (rt == 0) && (w == 0);
    const unsigned vz = lane * 16u;
    u32x2 Kh[5], Vh[5];
    auto attend = [&](const int bk) {
      const bool fm = first && bk == 0;
      const float sca = (fm && !cura) ? 0.f : SC, bia = (fm && !cura) ? NEG : 0.f;
      const float scb = (fm && !curb) ? 0.f : SC, bib = (fm && !curb) ? NEG : 0.f;
      f32x4 ox = {};
      const int tb = rt * 8 + 2 * w + bk;
      const u32x2 Qx = pk4(acc[4][bk]);
#pragma unroll
      for (int i = 0; i < 4; ++i) {
        const int a = (i + w) & 3;
        const unsigned mq = (g == a) ? 0xffffffffu : 0u, mv = (q == a) ? 0xffffffffu : 0u;
        const u32x2 Qm = pk4(acc[i][bk]);
        const u32x4 Qf = {Qm[0], Qm[1], Qx[0] & mq, Qx[1] & mq};
        const u32x2 kp = bk == 0 ? Kh[i] : Kp[i][0], kxp = bk == 0 ? Kh[4] : Kp[4][0];
        const u32x4 Kprev = {kp[0], kp[1], kxp[0], kxp[1]};
        const u32x4 Kcur = {Kp[i][bk][0], Kp[i][bk][1], Kp[4][bk][0], Kp[4][bk][1]};
        f32x4 s0 = mfmau(Kprev, Qf, f32x4{}), s1 = mfmau(Kcur, Qf, f32x4{});
        float s0a = par ? s0[1] : s0[0], s0b = par ? s0[3] : s0[2];
        float s1a = par ? s1[1] : s1[0], s1b = par ? s1[3] : s1[2];
        float la = (cura ? s1a : s0a) * sca + bia, lb = (curb ? s1b : s0b) * scb + bib;
        float m = xg_max(vmax(la, lb));
        float ea = __builtin_amdgcn_exp2f(la - m), eb = __builtin_amdgcn_exp2f(lb - m);
        float inv = __builtin_amdgcn_rcpf(xg_sum(ea + eb));
        unsigned ta = pk2(ea * inv, ea * inv), tbb = pk2(eb * inv, eb * inv);
        const u32x4 P = {ta & ma0, tbb & mb0, ta & ma1, tbb & mb1};
        const u32x2 vprev = bk == 0 ? Vh[i] : Vp[i][0], vxp = bk == 0 ? Vh[4] : Vp[4][0];
        f32x4 om = mfmau(u32x4{vprev[0], vprev[1], Vp[i][bk][0], Vp[i][bk][1]}, P, f32x4{});
        ox = mfmau(u32x4{vxp[0] & mv, vxp[1] & mv, Vp[4][bk][0] & mv, Vp[4][bk][1] & mv}, P, ox);
        *(u32x2*)(zb + (size_t)((tb * KS2 + 2 * G + (a >> 1)) * 1024 + 8 * (a & 1)) + (size_t)vz) = pk4(om);
      }
      *(u32x2*)(zb + (size_t)((tb * KS2 + 16 + (G >> 1)) * 1024 + 8 * (G & 1)) + (size_t)vz) = pk4(ox);
    };
    attend(1);
    LGKM_BAR();
#pragma unroll
    for (int i = 0; i < 5; ++i) {
      unsigned slot = i < 4 ? ((i + w) & 3) : 4;
      u32x4 v = *(LAS u32x4*)(size_t)(ex0 + w * 5120u + slot * 1024u);
      Kh[i] = u32x2{v[0], v[1]}; Vh[i] = u32x2{v[2], v[3]};
    }
    attend(0);
  } else {
    const int wr = w >> 1, wc = w & 1;
    const unsigned f = 2 * (g & 1) + (q >> 1);
    unsigned wa[6];
#pragma unroll
    for (int n = 0; n < 6; ++n) wa[n] = lds0 + lrow * 384u + 32u * ((6 * wc + n) ^ f) + 8u * pq;
    unsigned xa[2];
#pragma unroll
    for (int ks = 0; ks < 2; ++ks) xa[ks] = lds0 + M_WB + (64 * wr + li) * 128u + 16u * ((4 * ks + g) ^ (lane & 7));
    f32x4 acc[6][4];
    {
      const int L = ((idx - 16) * 4 + w) * 36 + (lane < 36 ? lane : 0);
      const char* pa; unsigned stride;
      if (L < 528) { int r = (xcd >> 1) * 512 - 16 + L; r = r < 0 ? 0 : r; pa = (const char*)p.x + r * 1280; stride = 128u; }
      else if (L < 1488) { const int t = L - 528, k = t / 15, j = t % 15; pa = (const char*)p.wqkv + k * 3840 + (j / 5) * 1280 + ch * 640 + (j % 5) * 128; stride = 245760u; }
      else { const int t = (L < 2256 ? L : 1488) - 1488, k = t / 12, j = t % 12; pa = (const char*)p.w1 + k * 3072 + ch * 1536 + j * 128; stride = 196608u; }
      pa += 3 * (size_t)stride;
#pragma unroll
      for (int st = 3; st < 10; ++st) { touch(pa, lds0 + K1_FLAGS + 2048u); pa += stride; }
    }
    bf16x8 wn[3], xn[4];
    unsigned wc_[6], xc_[2];
#define M_RDW(u) lds_tr8(wc_[(u) % 6] + (32 * ((u) / 6)) * 384u, wc_[(u) % 6] + (32 * ((u) / 6) + 8) * 384u)
#define M_SETADDR(sb) do { _Pragma("unroll") for (int i = 0; i < 6; ++i) { wc_[i] = wa[i] + (sb); OPAQUE(wc_[i]); } \
      _Pragma("unroll") for (int i = 0; i < 2; ++i) { xc_[i] = xa[i] + (sb); OPAQUE(xc_[i]); } } while (0)
    M_SETADDR(0u);
    LGKM_BAR();
    prog_wait_sleep(f_oth, 1);
    STAMP(1);
    wn[0] = M_RDW(0);
#pragma unroll
    for (int i = 0; i < 4; ++i) xn[i] = lds_b128(xc_[0] + 2048u * i);
    wn[1] = M_RDW(1); wn[2] = M_RDW(2);
#pragma unroll
    for (int n = 0; n < 6; ++n) {
      f32x4 bi = bf4_to_f32(*(LAS u32x2*)(size_t)(lds0 + M_BIAS + (96 * wc + 16 * n + 4 * g) * 2));
#pragma unroll
      for (int m = 0; m < 4; ++m) acc[n][m] = bi;
    }
    SB();
    unsigned sbn = M_STAGE;
#define M_ADR(u, t) (wc_[(u) % 6] + (32 * ((u) / 6) + 8 * (t)) * 384u)
    for (int st = 0; st < 10; ++st) {
      bf16x8 wf[12], xf[2][4];
      unsigned seen = 0;
#pragma unroll
      for (int i = 0; i < 3; ++i) wf[i] = wn[i];
#pragma unroll
      for (int i = 0; i < 4; ++i) xf[0][i] = xn[i];
#pragma unroll
      for (int u = 0; u < 12; ++u) {
        const int ks = u / 6, n = u % 6, tu = (u + 3) % 12;
        if (u == 8) {
          prog_wait(f_oth, seen, st < 9 ? st + 2 : 0);
          STAMPL(2 + st);
          SB();
        }
        acc[n][0] = mfma(wf[u], xf[ks][0], acc[n][0]);
        SB();
        const bf16x4 lo = lds_tr4(M_ADR(tu, 0));
        SB();
        acc[n][1] = mfma(wf[u], xf[ks][1], acc[n][1]);
        SB();
        const bf16x4 hi = lds_tr4(M_ADR(tu, 1));
        if (u < 9) wf[u + 3] = cat8(lo, hi); else wn[u - 9] = cat8(lo, hi);
        SB();
        acc[n][2] = mfma(wf[u], xf[ks][2], acc[n][2]);
        SB();
        if (u < 4) xf[1][u] = lds_b128(xc_[1] + 2048u * u);
        if (u == 5) seen = prog_read(f_oth);
        if (u >= 8) xn[u - 8] = lds_b128(xc_[0] + 2048u * (u - 8));
        SB();
        acc[n][3] = mfma(wf[u], xf[ks][3], acc[n][3]);
        SB();
        if (u >= 3 && u < 9) { wc_[u - 3] = wa[u - 3] + sbn; OPAQUE(wc_[u - 3]); }
        if (u == 1) { xc_[0] = xa[0] + sbn; OPAQUE(xc_[0]); }
        if (u == 9) { xc_[1] = xa[1] + sbn; OPAQUE(xc_[1]); }
        if (u == 8) prog_set(f_own, st + 1);
        if (u == 11) sbn = sbn == 2 * M_STAGE ? 0u : sbn + M_STAGE;
        SB();
      }
    }
    STAMP(12);
    const float C1 = -2.f * 0.7978845608028654f * 1.4426950408889634f, C2 = C1 * 0.044715f;
    const unsigned vz = lane * 16u;
#pragma unroll
    for (int m = 0; m < 4; ++m) {
      const int tb = rt * 8 + 4 * wr + m;
#pragma unroll
      for (int j = 0; j < 3; ++j) {
        u32x4 o;
#pragma unroll
        for (int e = 0; e < 2; ++e) {
#pragma unroll
          for (int k = 0; k < 2; ++k) {
            f32x2 x = {acc[2 * j + e][m][2 * k], acc[2 * j + e][m][2 * k + 1]};
            f32x2 t = x * (x * x * C2 + C1);
            f32x2 d = {__builtin_amdgcn_exp2f(t[0]), __builtin_amdgcn_exp2f(t[1])};
            d = d + 1.f;
            f32x2 r = {__builtin_amdgcn_rcpf(d[0]), __builtin_amdgcn_rcpf(d[1])};
            r = r * x;
            o[2 * e + k] = pk2(r[0], r[1]);
          }
        }
        *(u32x4*)(zb + (size_t)((tb * KS2 + 20 + 6 * G + 3 * wc + j) * 1024) + (size_t)vz) = o;
      }
    }
  }
  STAMP(13);
  STAMP_OUT(0);
}

constexpr unsigned K2_STAGE = 20480, K2_NB = 7, K2_FLAGS = K2_STAGE * K2_NB, K2_GB = K2_FLAGS + 2048 + 256, K2_LDS = K2_GB + 2048;
struct K2Args { const void* z; const void* wo; const void* w2; const void* b2; const float* gate; void* out; };

__global__ __launch_bounds__(512, 2) void k2(K2Args p) {
  extern __shared__ __attribute__((aligned(16))) char shm[];
  const unsigned lds0 = (unsigned)(uintptr_t)(LAS char*)shm;
  const int lane = threadIdx.x & 63;
  const int wid = __builtin_amdgcn_readfirstlane(threadIdx.x >> 6);
  const int b = blockIdx.x, xcd = b & 7, idx = b >> 3;
  const int rq = xcd >> 1, ch = xcd & 1;
  const int rti = idx % 6, cn = 5 * ch + idx / 6;
  const int tb0 = rq * 32 + rti * 6;
  const int nblk = rti < 5 ? 6 : 2;
  const unsigned f_own = lds0 + K2_FLAGS + wid * 256u + lane * 4u;
  const unsigned f_oth = lds0 + K2_FLAGS + (wid < 4 ? 1024u : 0u) + (lane & 3) * 260u;
  prog_set(f_own, 0u);
  STAMP_DECL
  STAMP(0); STAMP_RT(61);

  if (wid < 4) {
    const int lw = wid;
    const srd_t sz = make_srd(p.z, Z_BYTES);
    const srd_t so = make_srd(p.wo, D_ * D_ * 2);
    const srd_t s2 = make_srd(p.w2, F_ * D_ * 2);
    const unsigned rho = 8 * lw + (lane >> 3), t = rho >> 4, gg = (rho >> 2) & 3, qq = rho & 3;
    const unsigned cpos = lane & 7, sch = (((cpos >> 1) ^ ((rho >> 1) & 3)) << 1) | (cpos & 1);
    const unsigned cb = 128u * cn + 16u * sch;
    const unsigned vA = (20 * t + (rho & 15)) * 1280u + cb;
    const unsigned vB = (80 * t + 20 * gg + 16 + qq) * 1280u + cb;
    const unsigned vC = rho * 1280u + cb;
    const unsigned vz = lane * 16u;
    unsigned zo[3];
#pragma unroll
    for (int i = 0; i < 3; ++i) { int n = 3 * lw + i, blk = n >> 1; blk = blk < nblk ? blk : 0; zo[i] = (unsigned)(((tb0 + blk) * KS2 + (n & 1)) * 1024); }
    const srd_t sq = make_srd(g_seq, 256);
    const unsigned vzero = 0, f_row = lds0 + K2_FLAGS + wid * 256u;
    auto issue = [&](int st, unsigned lb, srd_t sw, unsigned vw, unsigned wo0, unsigned wstep) {
#pragma unroll
      for (int i = 0; i < 3; ++i) dma16(vz, sz, zo[i] + 2048u * st, lb + 1024u * (3 * lw + i));
      dma16(vw, sw, wo0, lb + 12288u + 1024u * lw);
      dma16(vw, sw, wo0 + wstep, lb + 16384u + 1024u * lw);
    };
    STAMP(1);
    issue(0, lds0, so, vA, 0u, 51200u);
    if (lw == 0) dma16(vz, make_srd(p.gate, S_ * 4), tb0 * 64u, lds0 + K2_GB);
    if (lw == 1) dma16(vz, make_srd(p.b2, D_ * 2), cn * 128u, lds0 + K2_GB + 1024u);
    LGKM_BAR();
    dma4(vzero, sq, 4, f_row);
#pragma unroll
    for (int st = 1; st < 7; ++st) { issue(st, lds0 + st * K2_STAGE, so, vA, 102400u * st, 51200u); dma4(vzero, sq, 4 * st + 4, f_row); }
    prog_wait_sleep(f_oth, 1);
    issue(7, lds0, so, vA, 102400u * 7, 51200u); dma4(vzero, sq, 32, f_row);
    prog_wait_sleep(f_oth, 2);
    issue(8, lds0 + K2_STAGE, so, vB, 0u, 204800u); dma4(vzero, sq, 36, f_row);
    prog_wait_sleep(f_oth, 3);
    issue(9, lds0 + 2 * K2_STAGE, so, vB, 409600u, 204800u); dma4(vzero, sq, 40, f_row);
    unsigned lb = lds0 + 3 * K2_STAGE;
    unsigned seen = prog_read(f_oth);
    for (int st = 10; st < 34; ++st) {
      if (__builtin_amdgcn_ballot_w64(seen < (unsigned)(st - (K2_NB - 1))) != 0) prog_wait_sleep(f_oth, st - (K2_NB - 1));
      seen = prog_read(f_oth);
      ISS(issue(SST(st), lb, s2, vC, 81920u * SST(st - 10), 40960u));
      dma4(vzero, sq, 4 * st + 4, f_row);
      STAMPL(st);
      lb = lb == lds0 + (K2_NB - 1) * K2_STAGE ? lds0 : lb + K2_STAGE;
    }
    STAMP(40);
    STAMP_OUT(1);
    return;
  }
  __builtin_amdgcn_s_setprio(2);
  const int w = wid - 4, wr = w >> 1, wc = w & 1;
  const int g = lane >> 4, q = (lane & 15) >> 2, pq = lane & 3, li = lane & 15;
  const unsigned za = lds0 + wr * 3 * 2048u + lane * 16u;
  unsigned wa[2];
  {
    const unsigned r = 4 * g + q;
#pragma unroll
    for (int n = 0; n < 2; ++n) wa[n] = lds0 + 12288u + r * 128u + 32u * ((2 * wc + n) ^ ((r >> 1) & 3)) + 8u * pq;
  }
  f32x4 acc[2][2][3];
#pragma unroll
  for (int h = 0; h < 2; ++h)
#pragma unroll
    for (int n = 0; n < 2; ++n)
#pragma unroll
      for (int m = 0; m < 3; ++m) acc[h][n][m] = f32x4{};
  bf16x8 zf[2][2][3], wf[2][2][2];
  unsigned zc = za, wc0 = wa[0], wc1 = wa[1], sbn = K2_STAGE;
  LGKM_BAR();
  prog_wait_sleep(f_oth, 1);
  STAMP(1);
  unsigned seen = prog_read(f_oth);
  SB();
#pragma unroll
  for (int e = 0; e < 2; ++e) {
#pragma unroll
    for (int m = 0; m < 3; ++m) zf[0][e][m] = lds_b128(zc + 2048u * m + 1024u * e);
    wf[0][e][0] = lds_tr8(wc0 + 4096u * e, wc0 + 4096u * e + 2048u);
    wf[0][e][1] = lds_tr8(wc1 + 4096u * e, wc1 + 4096u * e + 2048u);
  }
  prog_set(f_own, 1);
  auto stage = [&](const int st, const int pp, f32x4 (&a)[2][3]) {
    prog_wait(f_oth, seen, st < 33 ? st + 2 : 0);
    STAMPL(2 + st);
    SB();
    bf16x4 lo, hi;
#pragma unroll
    for (int e = 0; e < 2; ++e) {
      const unsigned eo = 4096u * e;
      MF(a[0][0], wf[pp][e][0], zf[pp][e][0]); SB();
      lo = lds_tr4(wc0 + eo); hi = lds_tr4(wc0 + eo + 2048u); wf[pp ^ 1][e][0] = cat8(lo, hi); SB();
      MF(a[0][1], wf[pp][e][0], zf[pp][e][1]); SB();
      zf[pp ^ 1][e][0] = lds_b128(zc + 1024u * e); SB();
      MF(a[0][2], wf[pp][e][0], zf[pp][e][2]); SB();
      zf[pp ^ 1][e][1] = lds_b128(zc + 2048u + 1024u * e); SB();
      MF(a[1][0], wf[pp][e][1], zf[pp][e][0]); SB();
      zf[pp ^ 1][e][2] = lds_b128(zc + 4096u + 1024u * e); SB();
      MF(a[1][1], wf[pp][e][1], zf[pp][e][1]); SB();
      lo = lds_tr4(wc1 + eo); hi = lds_tr4(wc1 + eo + 2048u); wf[pp ^ 1][e][1] = cat8(lo, hi); SB();
      MF(a[1][2], wf[pp][e][1], zf[pp][e][2]); SB();
      if (e == 0) seen = prog_read(f_oth);
      else {
        prog_set(f_own, st + 2);
        zc = za + sbn; wc0 = wa[0] + sbn; wc1 = wa[1] + sbn;
        OPAQUE(zc); OPAQUE(wc0); OPAQUE(wc1);
        sbn = sbn == (K2_NB - 1) * K2_STAGE ? 0u : sbn + K2_STAGE;
      }
      SB();
    }
  };
  zc = za + K2_STAGE; wc0 = wa[0] + K2_STAGE; wc1 = wa[1] + K2_STAGE; sbn = 2 * K2_STAGE;
  for (int st = 0; st < 10; st += 2) { stage(st, 0, acc[0]); stage(st + 1, 1, acc[0]); }
  for (int st = 10; st < 34; st += 2) { stage(st, 0, acc[1]); stage(st + 1, 1, acc[1]); }
  asm volatile("s_nop 7\n\ts_nop 7" ::: "memory");
  SB();
  STAMP(40);
  float gt[3];
  u32x2 bv[2];
#pragma unroll
  for (int m = 0; m < 3; ++m) gt[m] = *(LAS float*)(size_t)(lds0 + K2_GB + ((3 * wr + m) * 16 + li) * 4);
#pragma unroll
  for (int n = 0; n < 2; ++n) bv[n] = *(LAS u32x2*)(size_t)(lds0 + K2_GB + 1024u + (32 * wc + 16 * n + 4 * g) * 2);
#pragma unroll
  for (int m = 0; m < 3; ++m) {
    const int blk = 3 * wr + m;
    if (blk < nblk) {
      const int tok = (tb0 + blk) * 16 + li;
#pragma unroll
      for (int n = 0; n < 2; ++n) {
        const int col = 64 * cn + 32 * wc + 16 * n + 4 * g;
        f32x4 r = acc[0][n][m] * gt[m] + (acc[1][n][m] + bf4_to_f32(bv[n])) * (1.f - gt[m]);
        *(u32x2*)((char*)p.out + ((size_t)tok * D_ + col) * 2) = pk4(r);
      }
    }
  }
  STAMP(41);
  STAMP_OUT(1);
}

extern "C" void kernel_launch(void* const* d_in, const int* in_sizes, int n_in, void* d_out, int out_size, void* d_ws,
                              size_t ws_size, hipStream_t stream) {
  (void)in_sizes; (void)n_in; (void)out_size; (void)ws_size;
  (void)hipFuncSetAttribute((const void*)k1, hipFuncAttributeMaxDynamicSharedMemorySize, K1_LDS);
  (void)hipFuncSetAttribute((const void*)k2, hipFuncAttributeMaxDynamicSharedMemorySize, K2_LDS);
  char* ws = (char*)d_ws;
  K1Args a1{d_in[0], d_in[1], d_in[4], d_in[5], d_in[8], ws, (float*)(ws + Z_BYTES), d_in[2], d_in[6], d_out, d_in[7]};
  K2Args a2{ws, d_in[2], d_in[6], d_in[7], (const float*)(ws + Z_BYTES), d_out};
  k1<<<256, 512, K1_LDS, stream>>>(a1);
  k2<<<240, 512, K2_LDS, stream>>>(a2);
}
```

```cpp
#include <hip/hip_runtime.h>
#include <hip/hip_bf16.h>
#include <stdint.h>
#include <type_traits>

typedef __attribute__((ext_vector_type(8))) __bf16 bf16x8;
typedef __attribute__((ext_vector_type(4))) __bf16 bf16x4;
typedef __attribute__((ext_vector_type(2))) __bf16 bf16x2;
typedef __attribute__((ext_vector_type(4))) float f32x4;
typedef __attribute__((ext_vector_type(2))) float f32x2;
typedef __attribute__((ext_vector_type(4))) unsigned u32x4;
typedef __attribute__((ext_vector_type(2))) unsigned u32x2;
typedef __amdgpu_buffer_rsrc_t srd_t;
typedef __attribute__((address_space(1))) unsigned long long gu64;
typedef __attribute__((address_space(1))) unsigned gu32;
#define LAS __attribute__((address_space(3)))
#define DI __device__ __forceinline__
#define RLX_AGENT __ATOMIC_RELAXED, __HIP_MEMORY_SCOPE_AGENT
#define RLX_WG __ATOMIC_RELAXED, __HIP_MEMORY_SCOPE_WORKGROUP
#define SB() __builtin_amdgcn_sched_barrier(0)
#define OPAQUE(x) asm volatile("" : "+v"(x))
#define FENCE() asm volatile("" ::: "memory")
#define WAITV0() asm volatile("s_waitcnt vmcnt(0)" ::: "memory")
#define LGKM_BAR() asm volatile("s_waitcnt lgkmcnt(0)\n\ts_barrier" ::: "memory")

#define PFMODE 3
#define W0AT 10
#define PF2ST 0

constexpr int S_ = 2048, D_ = 640, F_ = 1536;
constexpr int KS2 = 68;
constexpr unsigned Z_BYTES = 128u * KS2 * 1024u;
constexpr unsigned WS_GATE = Z_BYTES, WS_FLAGS = WS_GATE + 8192, WS_EPOCH = WS_FLAGS + 2048;
constexpr unsigned SPIN_CAP = 1u << 18;
constexpr unsigned A_WB = 30720, A_STAGE = 49152;
constexpr unsigned M_WB = 24576, M_STAGE = 40960;
constexpr unsigned L_MISC = 3 * A_STAGE;
constexpr unsigned L_WROW = L_MISC + 2048;
constexpr unsigned L_SCR = L_WROW + 1024, L_GATE = L_SCR + 256, L_B2 = L_GATE + 1024, L_BIAS = L_B2 + 1024, L_GW = L_BIAS + 1024;
constexpr unsigned L_WORDS = L_GW + 2048, L_TOTAL = L_WORDS + 64;

struct Args { const void* x; const void* wqkv; const void* w1; const void* b1; const void* gw; const void* wo; const void* w2; const void* b2; void* out; char* ws; };

#define STAMP_DECL
#define STAMP(slot)
#define STAMPL(slot)
#define STAMP_RT(slot)
#define STAMP_OUT()
#define DIAG_ARG
#define DIAG_PASS

DI srd_t make_srd(const void* p, unsigned bytes) { return __builtin_amdgcn_make_buffer_rsrc((void*)p, (short)0, (int)bytes, 0x00020000); }
DI void dma16(unsigned voff, srd_t srd, unsigned soff, unsigned lds) {
  asm volatile("s_mov_b32 m0, %0\n\ts_nop 0\n\tbuffer_load_dwordx4 %1, %2, %3 offen lds" :: "s"(lds), "v"(voff), "s"(srd), "s"(soff) : "memory");
}
template <unsigned HI> DI void dma16m(unsigned voff, srd_t srd, unsigned soff, unsigned lds) {
  asm volatile("s_mov_b32 m0, %0\n\ts_mov_b32 exec_hi, %4\n\tbuffer_load_dwordx4 %1, %2, %3 offen lds\n\ts_mov_b32 exec_hi, -1"
               :: "s"(lds), "v"(voff), "s"(srd), "s"(soff), "i"(HI) : "memory");
}
DI void dma16c(unsigned voff, srd_t srd, unsigned soff, unsigned lds) {
  asm volatile("s_mov_b32 m0, %0\n\ts_nop 0\n\tbuffer_load_dwordx4 %1, %2, %3 offen sc1 lds" :: "s"(lds), "v"(voff), "s"(srd), "s"(soff) : "memory");
}
DI void dma16c1(unsigned voff, srd_t srd, unsigned soff, unsigned lds) {
  asm volatile("s_mov_b32 m0, %0\n\ts_mov_b64 exec, 1\n\tbuffer_load_dwordx4 %1, %2, %3 offen sc1 lds\n\ts_mov_b64 exec, -1"
               :: "s"(lds), "v"(voff), "s"(srd), "s"(soff) : "memory");
}
__device__ const unsigned g_seq[16] = {0, 1, 2, 3, 4, 5, 6, 7, 8, 9, 10, 11, 12, 13, 14, 15};
DI void dma4(unsigned voff, srd_t srd, unsigned soff, unsigned lds) {
  asm volatile("s_mov_b32 m0, %0\n\ts_mov_b64 exec, 1\n\tbuffer_load_dword %1, %2, %3 offen lds\n\ts_mov_b64 exec, -1"
               :: "s"(lds), "v"(voff), "s"(srd), "s"(soff) : "memory");
}
DI void touch(const char* addr, unsigned lds_scratch) {
  asm volatile("s_mov_b32 m0, %0\n\ts_nop 0\n\tglobal_load_lds_dword %1, off" :: "s"(lds_scratch), "v"(addr) : "memory");
}
DI bf16x4 lds_tr4(unsigned a) { return __builtin_amdgcn_ds_read_tr16_b64_v4bf16((LAS bf16x4*)(size_t)a); }
DI bf16x8 cat8(bf16x4 lo, bf16x4 hi) { return __builtin_shufflevector(lo, hi, 0, 1, 2, 3, 4, 5, 6, 7); }
DI bf16x8 lds_tr8(unsigned a0, unsigned a1) { return cat8(lds_tr4(a0), lds_tr4(a1)); }
DI bf16x8 lds_b128(unsigned a) { return *(LAS bf16x8*)(size_t)a; }
DI f32x4 mfma(bf16x8 a, bf16x8 b, f32x4 c) { return __builtin_amdgcn_mfma_f32_16x16x32_bf16(a, b, c, 0, 0, 0); }
DI f32x4 mfmau(u32x4 a, u32x4 b, f32x4 c) { return mfma(__builtin_bit_cast(bf16x8, a), __builtin_bit_cast(bf16x8, b), c); }
#define MF(c, a, b) asm volatile("v_mfma_f32_16x16x32_bf16 %0, %1, %2, %0" : "+v"(c) : "v"(a), "v"(b))
#define MF0(c, a, b) asm volatile("v_mfma_f32_16x16x32_bf16 %0, %1, %2, 0" : "=&v"(c) : "v"(a), "v"(b))
DI unsigned pk2(float a, float b) { bf16x2 v = {(__bf16)a, (__bf16)b}; return __builtin_bit_cast(unsigned, v); }
DI u32x2 pk4(f32x4 v) { return u32x2{pk2(v[0], v[1]), pk2(v[2], v[3])}; }
DI float vmax(float a, float b) { return __builtin_amdgcn_fmed3f(a, b, __builtin_inff()); }
DI f32x4 bf4_to_f32(u32x2 v) {
  return f32x4{__uint_as_float(v[0] << 16), __uint_as_float(v[0] & 0xffff0000u), __uint_as_float(v[1] << 16), __uint_as_float(v[1] & 0xffff0000u)};
}
DI void swap32(float& a, float& b) {
  auto r = __builtin_amdgcn_permlane32_swap(__float_as_uint(a), __float_as_uint(b), false, false);
  a = __uint_as_float(r[0]); b = __uint_as_float(r[1]);
}
DI void swap16(float& a, float& b) {
  auto r = __builtin_amdgcn_permlane16_swap(__float_as_uint(a), __float_as_uint(b), false, false);
  a = __uint_as_float(r[0]); b = __uint_as_float(r[1]);
}
template <int N, class F> DI void static_for(F&& f) {
  if constexpr (N > 0) { static_for<N - 1>(f); f(std::integral_constant<int, N - 1>{}); }
}

DI void prog_set(unsigned row_addr, unsigned v) { FENCE(); __hip_atomic_store((LAS unsigned*)(size_t)row_addr, v, RLX_WG); FENCE(); }
DI unsigned prog_read(unsigned addr) { return __hip_atomic_load((LAS unsigned*)(size_t)addr, RLX_WG); }
DI void prog_wait(unsigned addr, unsigned seen, unsigned need) {
  if (__builtin_expect(__builtin_amdgcn_ballot_w64(seen < need) != 0, 0)) {
    unsigned long long m;
    do {
      unsigned v;
      asm volatile("ds_read_b32 %0, %1\n\ts_waitcnt lgkmcnt(0)" : "=v"(v) : "v"(addr) : "memory");
      m = __builtin_amdgcn_ballot_w64(v < need);
    } while (m != 0);
  }
  FENCE();
}
DI void prog_wait_sleep(unsigned addr, unsigned need) {
  while (__builtin_amdgcn_ballot_w64(prog_read(addr) < need) != 0) __builtin_amdgcn_s_sleep(1);
  FENCE();
}
template <unsigned NEED> DI void prog_wait_inl(unsigned addr, unsigned seen) {
  asm volatile("v_cmp_gt_u32 vcc, %2, %0\n\ts_cbranch_vccz 2%=f\n1%=:\n\ts_sleep 1\n\tds_read_b32 %0, %1\n\ts_waitcnt lgkmcnt(0)\n\t"
               "v_cmp_gt_u32 vcc, %2, %0\n\ts_cbranch_vccnz 1%=b\n2%=:"
               : "+v"(seen) : "v"(addr), "i"(NEED) : "vcc", "memory");
}
DI unsigned swz23(unsigned k) { return (k & ~12u) | ((k & 4u) << 1) | ((k & 8u) >> 1); }

DI unsigned long long get_epoch(const unsigned lds0) { return *(LAS unsigned long long*)(size_t)(lds0 + L_WORDS + 16); }
DI unsigned long long get_tag(const unsigned lds0) { return (get_epoch(lds0) + 1) * 0x9E3779B97F4A7C15ull; }
DI void publish(const Args& p, const unsigned lds0, const int lane, const int fidx, const unsigned last) {
  WAITV0();
  if (lane == 0) {
    const unsigned old = __hip_atomic_fetch_add((LAS unsigned*)(size_t)(lds0 + L_WORDS), 1u, RLX_WG);
    if (old == last) __hip_atomic_store((gu64*)(p.ws + WS_FLAGS) + fidx, get_tag(lds0), RLX_AGENT);
  }
  FENCE();
}
DI void prefetch_w2(const Args& p, const unsigned lds0, const int lane, const int wv128, const int ch) {
#pragma unroll
  for (int j = 0; j < 2; ++j) {
    int L = (j * 128 + wv128) * 43 + lane;
    L = (L < 10880 && lane < 43) ? L : 0;
    const int r = L / 5, cl = L % 5;
    touch((r < 1536 ? (const char*)p.w2 + r * 1280 : (const char*)p.wo + (r - 1536) * 1280) + ch * 640 + cl * 128, lds0 + L_SCR);
  }
}

constexpr int NW2 = 18;
template <int NB> constexpr int zdepth() { return NB == 6 ? 8 : 12; }

template <int NB>
DI void loader2(const Args& p, const unsigned lds0, const int lane, const int lw, const int tb0, const int cn, const unsigned f_cmp DIAG_ARG) {
  const unsigned f_oth = (lane & 4) ? lds0 + L_WORDS + 8 : f_cmp;
  const srd_t so = make_srd(p.wo, D_ * D_ * 2);
  const srd_t s2 = make_srd(p.w2, F_ * D_ * 2);
  const unsigned rho = 8 * lw + (lane >> 3), t = rho >> 4, gg = (rho >> 2) & 3, qq = rho & 3;
  const unsigned cpos = lane & 7, sch = (((cpos >> 1) ^ ((rho >> 1) & 3)) << 1) | (cpos & 1);
  const unsigned cb = 128u * cn + 16u * sch;
  const unsigned vA = (20 * t + (rho & 15)) * 1280u + cb;
  const unsigned vB = (80 * t + 20 * gg + 16 + qq) * 1280u + cb;
  const unsigned vC = rho * 1280u + cb;
  const unsigned vz = lane * 16u;
  const unsigned wl = lds0 + 1024u * lw;
  const unsigned f_w = lds0 + L_WROW + lw * 256u + lane * 4u;
  u32x4 wreg[2 * (34 - NW2)];
  auto load_wreg = [&]() {
#pragma unroll
    for (int k = 2 * NW2; k < 68; ++k)
      wreg[k - 2 * NW2] = k < 48 ? __builtin_amdgcn_raw_buffer_load_b128(s2, vC, 40960 * k, 0)
                        : (k < 64 ? __builtin_amdgcn_raw_buffer_load_b128(so, vA, 51200 * (k - 48), 0)
                                  : __builtin_amdgcn_raw_buffer_load_b128(so, vB, 204800 * (k - 64), 0));
  };
  if (lw == 1) dma16(vz, make_srd(p.b2, D_ * 2), cn * 128u, lds0 + L_B2);
  prog_wait_sleep(f_oth, W0AT);
  for (int k = 0; k < 24; ++k) dma16(vC, s2, 40960u * k, wl + 49152u + 4096u * k);
  WAITV0();
  prog_set(f_w, 10 + 12);
  const unsigned long long tag = get_tag(lds0);
  const gu64* fl = (const gu64*)(p.ws + WS_FLAGS) + (((lane & 16) ? (tb0 + NB - 1) : tb0) >> 3) * 16 + (lane & 15);
  int last = 0;
  auto poll = [&]() {
    const unsigned long long v = __hip_atomic_load(fl, RLX_AGENT);
    const unsigned long long m = __builtin_amdgcn_ballot_w64(v == tag);
    const int n = __builtin_ctz(~((unsigned)m & (unsigned)(m >> 16) & 0xffffu));
    const int pub = n == 16 ? 63 : (n <= 8 ? 3 * n : 16 + n) + 11 - zdepth<NB>();
    if (pub > last) {
      last = pub;
      FENCE();
      __hip_atomic_store((LAS unsigned*)(size_t)(lds0 + L_WORDS + 4), (unsigned)pub, RLX_WG);
      FENCE();
    }
  };
  while (__builtin_amdgcn_readfirstlane(prog_read(lds0 + L_WORDS)) < (NB == 4 ? 4u : 5u)) {
    if (lw == 0 && last < 35 - zdepth<NB>()) poll(); else __builtin_amdgcn_s_sleep(2);
  }
  FENCE();
  if (lw == 0) {
#pragma nounroll
    for (unsigned spins = 0; spins < SPIN_CAP && last < 11; ++spins) poll();
  }
  for (int k = 24; k < 36; ++k) dma16(vC, s2, 40960u * k, wl + 4096u * (k - 24));
  load_wreg();
  STAMP(16); STAMP_RT(56);
  if (lw == 0) {
    poll();
    prog_set(f_w, 10 + NW2);
#pragma nounroll
    for (unsigned spins = 0; spins < SPIN_CAP && last < 63; ++spins) { __builtin_amdgcn_s_sleep(1); poll(); }
    FENCE();
    __hip_atomic_store((LAS unsigned*)(size_t)(lds0 + L_WORDS + 4), 63u, RLX_WG);
    dma16c(vz, make_srd(p.ws + WS_GATE, S_ * 4), tb0 * 64u, lds0 + L_GATE);
  }
  __builtin_amdgcn_s_waitcnt(0x0F70);
  prog_set(f_w, 10 + NW2);
  STAMP(17); STAMP_RT(57);
  const unsigned wv = wl + vz;
  unsigned seen = prog_read(f_cmp);
  static_for<34 - NW2>([&](auto jc) {
    constexpr int j = decltype(jc)::value;
    prog_wait_inl<13 + j>(f_cmp, seen);
    seen = prog_read(f_cmp);
    *(LAS u32x4*)(size_t)(wv + ((j + 6) % NW2) * 8192u) = wreg[2 * j];
    *(LAS u32x4*)(size_t)(wv + ((j + 6) % NW2) * 8192u + 4096u) = wreg[2 * j + 1];
    prog_set(f_w, 11 + NW2 + j);
  });
  if (blockIdx.x == 0 && lw == 0) {
    const gu64* fa = (const gu64*)(p.ws + WS_FLAGS) + lane;
#pragma nounroll
    for (unsigned spins = 0; spins < SPIN_CAP; ++spins) {
      bool ok = true;
#pragma unroll
      for (int j = 0; j < 4; ++j) ok &= __hip_atomic_load(fa + 64 * j, RLX_AGENT) == tag;
      if (__builtin_amdgcn_ballot_w64(!ok) == 0) break;
      __builtin_amdgcn_s_sleep(2);
    }
    if (lane == 0) __hip_atomic_store((gu64*)(p.ws + WS_EPOCH), get_epoch(lds0) + 1, RLX_AGENT);
  }
}

template <int NB>
DI void compute2(const Args& p, const unsigned lds0, const int lane, const int w, const int tb0, const int cn, const unsigned f_own DIAG_ARG) {
  constexpr int MB = NB / 2, ZD = zdepth<NB>();
  const unsigned f_oth = (lane & 4) ? lds0 + L_WORDS + 4 : lds0 + L_WROW + (lane & 3) * 260u;
  const unsigned f_cmp = lds0 + L_MISC + 1024u + (lane & 3) * 260u;
  const int wr = w >> 1, e = w & 1;
  const int g = lane >> 4, q = (lane & 15) >> 2, pq = lane & 3, li = lane & 15;
  const srd_t sz = make_srd(p.ws, Z_BYTES);
  unsigned zv[MB];
#pragma unroll
  for (int m = 0; m < MB; ++m) zv[m] = (unsigned)(((tb0 + MB * wr + m) * KS2 + e) * 1024) + lane * 16u;
  unsigned wa[3][4];
  {
    const unsigned r = 4 * g + q;
#pragma unroll
    for (int n = 0; n < 4; ++n) {
      wa[0][n] = lds0 + e * 4096u + r * 128u + 32u * (n ^ ((r >> 1) & 3)) + 8u * pq;
      wa[1][n] = wa[0][n] + 49152u; wa[2][n] = wa[0][n] + 98304u;
      OPAQUE(wa[0][n]); OPAQUE(wa[1][n]); OPAQUE(wa[2][n]);
    }
  }
  f32x4 acc[2][4][MB];
  u32x4 zf[ZD][MB];
  bf16x8 wf[2][4];
#define C2_ZLD(s, m) __builtin_amdgcn_raw_buffer_load_b128(sz, zv[m], ((s) < 24 ? 20 + 2 * (s) : 2 * (s) - 48) * 1024, 16)
#define C2_WAD(s, n) (wa[(((s) + 6) % NW2) / 6][n] + ((((s) + 6) % NW2) % 6) * 8192u)
  prog_wait_sleep(f_oth, 11);
  STAMP(20); STAMP_RT(57);
#pragma unroll
  for (int s = 0; s < ZD; ++s) {
#pragma unroll
    for (int m = 0; m < MB; ++m) zf[s][m] = C2_ZLD(s, m);
    SB();
  }
  unsigned seen = prog_read(f_oth);
  SB();
#pragma unroll
  for (int n = 0; n < 4; ++n) wf[0][n] = lds_tr8(C2_WAD(0, n), C2_WAD(0, n) + 2048u);
  prog_set(f_own, 13);
  unsigned one = 1; OPAQUE(one);
  static_for<34>([&](auto sc) {
    constexpr int st = decltype(sc)::value, pp = st & 1, h = st < 24 ? 1 : 0, sn = st < 33 ? st + 1 : 33;
    if constexpr (st < 33) prog_wait_inl<st + 12>(f_oth, seen);
    STAMPL(21 + st);
    SB();
    bf16x4 lo[4], hi[4];
    u32x4 zc[MB];
#pragma unroll
    for (int m = 0; m < MB; ++m) zc[m] = zf[st % ZD][m];
#define C2_LO(n) lo[n] = lds_tr4(C2_WAD(sn, n))
#define C2_HI(n) do { hi[n] = lds_tr4(C2_WAD(sn, n) + 2048u); wf[pp ^ 1][n] = cat8(lo[n], hi[n]); } while (0)
#pragma unroll
    for (int k = 0; k < 4 * MB; ++k) {
      const int n = k / MB, m = k % MB;
      if (st == 0 || st == 24) MF0(acc[h][n][m], wf[pp][n], zc[m]); else MF(acc[h][n][m], wf[pp][n], zc[m]);
      SB();
      if (st < 33) {
        if (k == 0) { C2_LO(0); seen = prog_read(f_oth); }
        if (k == 1) C2_HI(0);
        if (k == 2) C2_LO(1);
        if (k == 3) C2_HI(1);
        if (k == 4) C2_LO(2);
        if (k == 5) C2_HI(2);
        if (k == 6) C2_LO(3);
        if (k == 7) { C2_HI(3); FENCE(); __hip_atomic_fetch_add((LAS unsigned*)(size_t)f_own, one, RLX_WG); FENCE(); }
      }
      if (n == 3 && st + ZD < 34) zf[st % ZD][m] = C2_ZLD(st + ZD, m);
      SB();
    }
  });
  float gt[MB];
#pragma unroll
  for (int m = 0; m < MB; ++m) gt[m] = *(LAS float*)(size_t)(lds0 + L_GATE + ((MB * wr + m) * 16 + li) * 4);
  const unsigned seen2 = prog_read(f_cmp);
  asm volatile("s_nop 7\n\ts_nop 7" ::: "memory");
  SB();
  STAMP(58);
  prog_wait(f_cmp, seen2, 13 + 25);
  constexpr unsigned HALF = NB * 16 * 272;
  const unsigned xw = lds0 + 49152u + e * HALF + (wr * MB * 16 + li) * 272u + g * 16u;
#pragma unroll
  for (int m = 0; m < MB; ++m)
#pragma unroll
    for (int n = 0; n < 4; ++n) *(LAS f32x4*)(size_t)(xw + m * 16 * 272u + n * 64u) = acc[0][n][m] * gt[m] + acc[1][n][m] * (1.f - gt[m]);
  prog_set(f_own, 50);
  const int r0 = w * (NB * 4) + (lane >> 3);
  const u32x4 bv = *(LAS u32x4*)(size_t)(lds0 + L_B2 + (lane & 7) * 16u);
  float g2[NB / 2];
#pragma unroll
  for (int j = 0; j < NB / 2; ++j) g2[j] = 1.f - *(LAS float*)(size_t)(lds0 + L_GATE + (r0 + 8 * j) * 4);
  prog_wait(f_cmp, prog_read(f_cmp), 50);
  const unsigned xr = lds0 + 49152u + r0 * 272u + (lane & 7) * 32u;
  char* ob = (char*)p.out + (size_t)(tb0 * 16 * D_ + 64 * cn) * 2;
  const unsigned vo = (r0 * D_ + (lane & 7) * 8) * 2;
  const f32x4 b0 = bf4_to_f32(u32x2{bv[0], bv[1]}), b1 = bf4_to_f32(u32x2{bv[2], bv[3]});
#pragma unroll
  for (int j = 0; j < NB / 2; ++j) {
    const unsigned a = xr + j * 8 * 272u;
    const f32x4 lo = *(LAS f32x4*)(size_t)a + *(LAS f32x4*)(size_t)(a + HALF) + b0 * g2[j];
    const f32x4 hi = *(LAS f32x4*)(size_t)(a + 16u) + *(LAS f32x4*)(size_t)(a + HALF + 16u) + b1 * g2[j];
    const u32x2 pl = pk4(lo), ph = pk4(hi);
    *(u32x4*)(ob + (size_t)(j * 8 * D_ * 2) + (size_t)vo) = u32x4{pl[0], pl[1], ph[0], ph[1]};
  }
}

__global__ __launch_bounds__(512, 2) void fused(Args p) {
  extern __shared__ __attribute__((aligned(16))) char shm[];
  const unsigned lds0 = (unsigned)(uintptr_t)(LAS char*)shm;
  const int lane = threadIdx.x & 63;
  const int wid = __builtin_amdgcn_readfirstlane(threadIdx.x >> 6);
  const int b = blockIdx.x, xcd = b & 7, idx = b >> 3;
  const int rt = (xcd >> 1) * 4 + (idx & 3), c = idx >> 2, ch = xcd & 1;
  const bool is_attn = c < 4;
  const int G = 4 * ch + (c & 3);
  const int T0 = rt * 128;
  const int xi = idx & 15;
  const int fidx = rt * 16 + (is_attn ? 8 + G : G);
  const unsigned f_own = lds0 + L_MISC + wid * 256u + lane * 4u;
  const unsigned f_oth = lds0 + L_MISC + (wid < 4 ? 1024u : 0u) + (lane & 3) * 260u;
  prog_set(f_own, 0u);
  STAMP_DECL
  STAMP(0); STAMP_RT(61);

  if (wid < 4) {
    const int lw = wid;
    const srd_t sx = make_srd(p.x, S_ * D_ * 2);
    const srd_t sq = make_srd(g_seq, 64);
    const unsigned vzero = 0, f_row = lds0 + L_MISC + wid * 260u;
    const unsigned vx = (lane >> 3) * 1280u + 16u * ((lane & 7) ^ (lane >> 3));
    prog_set(lds0 + L_WROW + lw * 256u + lane * 4u, 0u);
    if (threadIdx.x == 0) *(LAS u32x4*)(size_t)(lds0 + L_WORDS) = u32x4{0u, 0u, is_attn ? 63u : 0u, 0u};
    if (is_attn) {
      const srd_t sw = make_srd(p.wqkv, D_ * 3 * D_ * 2);
      const unsigned r2 = lane >= 30 ? 1u : 0u, rem = lane - 30u * r2, sec = (rem >= 20 ? 2u : (rem >= 10 ? 1u : 0u));
      const unsigned vw = r2 * 3840u + sec * 1280u + 16u * (rem - 10u * sec);
      const int hr = 4 * lw + (lane >> 3);
      const int ht = T0 - 16 + hr;
      const unsigned vh = (unsigned)(ht < 0 ? 0 : ht) * 1280u + 16u * ((lane & 7) ^ (hr & 7));
      auto issue = [&](int st, unsigned lb) {
        const unsigned sow = st * 245760u + 160u * G, sox = st * 128u + (unsigned)T0 * 1280u;
        dma16m<0u>(vh, sx, st * 128u, lb + A_WB + 512u * lw);
#pragma unroll
        for (int i = 0; i < 4; ++i) { const unsigned n = 4 * lw + i; dma16(vx, sx, sox + n * 10240u, lb + A_WB + 2048u + 1024u * n); }
#pragma unroll
        for (int i = 0; i < 8; ++i) { const unsigned n = 8 * lw + i; dma16m<0x0fffffffu>(vw, sw, sow + swz23(2 * n) * 3840u, lb + 960u * n); }
      };
      STAMP(1);
      issue(0, lds0);
      if (lw == 0) dma16c1(vzero, make_srd(p.ws + WS_EPOCH, 16), 0u, lds0 + L_WORDS + 16);
      LGKM_BAR();
      dma4(vzero, sq, 4, f_row);
      unsigned lb = lds0 + A_STAGE;
      for (int st = 1; st < 10; ++st) {
        if (st >= 3) prog_wait_sleep(f_oth, st - 2);
        issue(st, lb);
        dma4(vzero, sq, 4 * st + 4, f_row);
        if (PF2ST > 0 && st == PF2ST) prefetch_w2(p, lds0, lane, idx * 4 + lw, ch);
        STAMPL(1 + st);
        lb = lb == lds0 + 2 * A_STAGE ? lds0 : lb + A_STAGE;
      }
      STAMP(15);
      int cj, blk0;
      if (xi < 6) { cj = xi >> 1; blk0 = 24 + 4 * (xi & 1); } else { const int t = xi - 6; cj = t < 5 ? 3 : 4; blk0 = 12 + 4 * (t < 5 ? t : t - 5); }
      loader2<4>(p, lds0, lane, lw, (xcd >> 1) * 32 + blk0, 5 * ch + cj, f_oth DIAG_PASS);
    } else {
      const srd_t sw = make_srd(p.w1, D_ * F_ * 2);
      unsigned vw[3];
#pragma unroll
      for (int i = 0; i < 3; ++i) {
        const unsigned cw = 64 * i + lane, j = (cw * 171u) >> 12, cp = cw - 24u * j;
        vw[i] = (8u * (j >> 2) + (j & 3)) * 3072u + 32u * ((cp >> 1) ^ ((j >> 1) & 3)) + 16u * (cp & 1);
      }
      auto issue = [&](int st, unsigned lb) {
        const unsigned sow = st * 196608u + 384u * G + (16u * (lw >> 1) + 4u * (lw & 1)) * 3072u, sox = st * 128u + (unsigned)T0 * 1280u;
#pragma unroll
        for (int i = 0; i < 4; ++i) { const unsigned n = 4 * lw + i; dma16(vx, sx, sox + n * 10240u, lb + M_WB + 1024u * n); }
#pragma unroll
        for (int i = 0; i < 3; ++i) dma16(vw[i], sw, sow, lb + 3072u * lw + 1024u * i);
#pragma unroll
        for (int i = 0; i < 3; ++i) dma16(vw[i], sw, sow + 32u * 3072u, lb + 3072u * (lw + 4) + 1024u * i);
      };
      STAMP(1);
      issue(0, lds0);
      if (lw == 0) dma16c1(vzero, make_srd(p.ws + WS_EPOCH, 16), 0u, lds0 + L_WORDS + 16);
      if (lw == 1) dma16(lane * 16u, make_srd(p.b1, F_ * 2), 384u * G, lds0 + L_BIAS);
      const bool gw_wave = lw == (G >> 1);
      if (gw_wave) { const srd_t sg = make_srd(p.gw, D_ * 2); dma16(lane * 16u, sg, 0u, lds0 + L_GW); dma16(lane * 16u, sg, 1024u, lds0 + L_GW + 1024u); }
      LGKM_BAR();
      dma4(vzero, sq, 4, f_row);
      const unsigned gxa = M_WB + 2048u * G + lane * 16u, gwa = lds0 + L_GW + 16u * ((lane & 7) ^ (lane >> 3));
      float ga0 = 0.f, ga1 = 0.f;
      auto gate_step = [&](const u32x4 x0, const u32x4 x1, const u32x4 gv) {
#pragma unroll
        for (int e = 0; e < 4; ++e) {
          const float gl = __uint_as_float(gv[e] << 16), gh = __uint_as_float(gv[e] & 0xffff0000u);
          ga0 += __uint_as_float(x0[e] << 16) * gl; ga0 += __uint_as_float(x0[e] & 0xffff0000u) * gh;
          ga1 += __uint_as_float(x1[e] << 16) * gl; ga1 += __uint_as_float(x1[e] & 0xffff0000u) * gh;
        }
      };
      unsigned lb = lds0 + M_STAGE;
      for (int st = 1; st < 10; ++st) {
        u32x4 gx0 = {}, gx1 = {}, gv = {};
        if (st >= 3) {
          prog_wait_sleep(f_oth, st - 2);
          if (gw_wave) {
            gx0 = *(LAS u32x4*)(size_t)(lb + gxa); gx1 = *(LAS u32x4*)(size_t)(lb + gxa + 1024u); gv = *(LAS u32x4*)(size_t)(gwa + 128u * (st - 3));
            asm volatile("s_waitcnt lgkmcnt(0)" : "+v"(gx0), "+v"(gx1), "+v"(gv) :: "memory");
          }
        }
        issue(st, lb);
        dma4(vzero, sq, 4 * st + 4, f_row);
        if (PF2ST > 0 && st == PF2ST) prefetch_w2(p, lds0, lane, idx * 4 + lw, ch);
        if (gw_wave) gate_step(gx0, gx1, gv);
        STAMPL(1 + st);
        lb = lb == lds0 + 2 * M_STAGE ? lds0 : lb + M_STAGE;
      }
      if (gw_wave) {
        for (int st = 7; st < 10; ++st) {
          while (__builtin_amdgcn_readfirstlane(prog_read(f_row)) < (unsigned)(st + 1)) __builtin_amdgcn_s_sleep(1);
          FENCE();
          const u32x4 gx0 = *(LAS u32x4*)(size_t)(lb + gxa), gx1 = *(LAS u32x4*)(size_t)(lb + gxa + 1024u), gv = *(LAS u32x4*)(size_t)(gwa + 128u * st);
          FENCE();
          __hip_atomic_store((LAS unsigned*)(size_t)(lds0 + L_WORDS + 8), st < 9 ? (unsigned)(st + 1) : 63u, RLX_WG);
          FENCE();
          gate_step(gx0, gx1, gv);
          lb = lb == lds0 + 2 * M_STAGE ? lds0 : lb + M_STAGE;
        }
#pragma unroll
        for (int o = 4; o > 0; o >>= 1) { ga0 += __shfl_xor(ga0, o); ga1 += __shfl_xor(ga1, o); }
        const float gate = __builtin_amdgcn_rcpf(1.f + __expf(-((lane & 1) ? ga1 : ga0)));
        if ((lane & 6) == 0) __hip_atomic_store((gu32*)(p.ws + WS_GATE) + T0 + 16 * G + 8 * (lane & 1) + (lane >> 3), __float_as_uint(gate), RLX_AGENT);
        publish(p, lds0, lane, fidx, 4);
      }
      STAMP(15);
      int cj, blk0;
      if (xi < 12) { cj = xi >> 2; blk0 = 6 * (xi & 3); } else { cj = 3 + ((xi - 12) >> 1); blk0 = 6 * (xi & 1); }
      loader2<6>(p, lds0, lane, lw, (xcd >> 1) * 32 + blk0, 5 * ch + cj, f_oth DIAG_PASS);
    }
    STAMP_OUT();
    return;
  }

  const int w = wid - 4;
  LGKM_BAR();
  if (is_attn == (PFMODE == 3)) {
    const char* pa; unsigned pstride;
    const int L = (xi * 4 + w) * 36 + (lane < 36 ? lane : 0);
    if (L < 528) { int r = (xcd >> 1) * 512 - 16 + L; r = r < 0 ? 0 : r; pa = (const char*)p.x + r * 1280; pstride = 128u; }
    else if (L < 1488) { const int t = L - 528, k = t / 15, j = t % 15; pa = (const char*)p.wqkv + k * 3840 + (j / 5) * 1280 + ch * 640 + (j % 5) * 128; pstride = 245760u; }
    else { const int t = (L < 2256 ? L : 1488) - 1488, k = t / 12, j = t % 12; pa = (const char*)p.w1 + k * 3072 + ch * 1536 + j * 128; pstride = 196608u; }
    pa += 3 * (size_t)pstride;
#pragma unroll
    for (int st = 3; st < 10; ++st) { touch(pa, lds0 + L_SCR); pa += pstride; }
  }
  __builtin_amdgcn_s_setprio(2);
  const int g = lane >> 4, q = (lane & 15) >> 2, pq = lane & 3, li = lane & 15;
  const unsigned lrow = 16 * (g >> 1) + 4 * (g & 1) + q;
  const srd_t sz = make_srd(p.ws, Z_BYTES);
  const unsigned vz = lane * 16u;
  if (is_attn) {
    unsigned wa[5];
#pragma unroll
    for (int i = 0; i < 4; ++i) wa[i] = lds0 + lrow * 480u + 40u * (i ^ w) + 8u * pq;
    wa[4] = lds0 + lrow * 480u + 40u * pq + 32u;
    unsigned xa[2], xh[2];
#pragma unroll
    for (int ks = 0; ks < 2; ++ks) {
      xh[ks] = lds0 + A_WB + li * 128u + 16u * ((4 * ks + g) ^ (lane & 7));
      xa[ks] = xh[ks] + 2048u * (1 + 2 * w);
    }
    f32x4 acc[15][2], hk0 = {}, hkx = {}, hv0 = {}, hvx = {};
#pragma unroll
    for (int u = 0; u < 15; ++u) { acc[u][0] = f32x4{}; acc[u][1] = f32x4{}; }
    bf16x8 wn[6], xn[3];
    unsigned wc_[5], xc_[2], hc_[2];
#define A_ADR(u, t) (wc_[(u) % 5] + (32 * ((u) / 15) + 8 * (t)) * 480u + (((u) % 15) / 5) * 160u)
#pragma unroll
    for (int i = 0; i < 5; ++i) { wc_[i] = wa[i]; OPAQUE(wc_[i]); }
#pragma unroll
    for (int i = 0; i < 2; ++i) { xc_[i] = xa[i]; hc_[i] = xh[i]; OPAQUE(xc_[i]); OPAQUE(hc_[i]); }
    prog_wait_sleep(f_oth, 1);
    STAMP(1);
    xn[0] = lds_b128(xc_[0]); xn[1] = lds_b128(xc_[0] + 2048u);
#pragma unroll
    for (int i = 0; i < 6; ++i) wn[i] = lds_tr8(A_ADR(i, 0), A_ADR(i, 1));
    xn[2] = lds_b128(hc_[0]);
    SB();
    unsigned sbn = A_STAGE;
    for (int st = 0; st < 10; ++st) {
      bf16x8 wf[30], xf[2][3];
      unsigned seen = 0;
#pragma unroll
      for (int i = 0; i < 6; ++i) wf[i] = wn[i];
#pragma unroll
      for (int i = 0; i < 3; ++i) xf[0][i] = xn[i];
#pragma unroll
      for (int u = 0; u < 30; ++u) {
        const int ks = u / 15, uu = u % 15, sec = uu / 5, tu = (u + 6) % 30;
        if (u == 24) {
          prog_wait(f_oth, seen, st < 9 ? st + 2 : 0);
          STAMPL(2 + st);
          SB();
        }
        if (sec < 2) acc[uu][0] = mfma(wf[u], xf[ks][0], acc[uu][0]); else acc[uu][0] = mfma(xf[ks][0], wf[u], acc[uu][0]);
        SB();
        const bf16x4 lo = lds_tr4(A_ADR(tu, 0));
        SB();
        if (sec < 2) acc[uu][1] = mfma(wf[u], xf[ks][1], acc[uu][1]); else acc[uu][1] = mfma(xf[ks][1], wf[u], acc[uu][1]);
        SB();
        const bf16x4 hi = lds_tr4(A_ADR(tu, 1));
        if (u < 24) wf[u + 6] = cat8(lo, hi); else wn[u - 24] = cat8(lo, hi);
        if (u >= 19 && u < 24) { wc_[u - 19] = wa[u - 19] + sbn; OPAQUE(wc_[u - 19]); }
        if (u == 12) { xc_[0] = xa[0] + sbn; OPAQUE(xc_[0]); }
        if (u == 13) { hc_[0] = xh[0] + sbn; OPAQUE(hc_[0]); }
        if (u == 16) { xc_[1] = xa[1] + sbn; OPAQUE(xc_[1]); }
        if (u == 17) { hc_[1] = xh[1] + sbn; OPAQUE(hc_[1]); }
        if (u == 23) prog_set(f_own, st + 1);
        SB();
        if (uu == 5) hk0 = mfma(wf[u], xf[ks][2], hk0);
        if (uu == 9) hkx = mfma(wf[u], xf[ks][2], hkx);
        if (uu == 10) hv0 = mfma(xf[ks][2], wf[u], hv0);
        if (uu == 14) hvx = mfma(xf[ks][2], wf[u], hvx);
        SB();
        if (u == 5) xf[1][0] = lds_b128(xc_[1]);
        if (u == 9) xf[1][1] = lds_b128(xc_[1] + 2048u);
        if (u == 10) xf[1][2] = lds_b128(hc_[1]);
        if (u == 20) seen = prog_read(f_oth);
        if (u == 24) xn[0] = lds_b128(xc_[0]);
        if (u == 25) xn[1] = lds_b128(xc_[0] + 2048u);
        if (u == 29) { xn[2] = lds_b128(hc_[0]); sbn = sbn == 2 * A_STAGE ? 0u : sbn + A_STAGE; }
        SB();
      }
    }
    STAMP(12);
    const unsigned f_cmp = f_oth + 1024u;
    const unsigned seen10 = prog_read(f_cmp);
    u32x4 Kt[4][2];
    u32x2 Vp[5][2];
#pragma unroll
    for (int bk = 1; bk >= 0; --bk) {
#pragma unroll
      for (int i = 0; i < 4; ++i) {
        const u32x2 km = pk4(acc[5 + i][bk]), kx = pk4(acc[9][bk]);
        Kt[i][bk] = u32x4{km[0], km[1], kx[0], kx[1]};
      }
#pragma unroll
      for (int i = 0; i < 5; ++i) Vp[i][bk] = pk4(acc[10 + i][bk]);
      if (bk == 1) {
        prog_wait(f_cmp, seen10, 10);
        const unsigned ek = lds0 + lane * 16u, ev = lds0 + 4096u + lane * 8u;
        if (w < 3) {
#pragma unroll
          for (int i = 0; i < 4; ++i) {
            *(LAS u32x4*)(size_t)(ek + (w + 1) * 8192u + (i ^ w) * 1024u) = Kt[i][1];
            *(LAS u32x2*)(size_t)(ev + (w + 1) * 8192u + (i ^ w) * 512u) = Vp[i][1];
          }
          *(LAS u32x2*)(size_t)(ev + (w + 1) * 8192u + 2048u) = Vp[4][1];
        }
        const u32x2 a = pk4(hk0), ax = pk4(hkx);
        *(LAS u32x4*)(size_t)(ek + w * 1024u) = u32x4{a[0], a[1], ax[0], ax[1]};
        *(LAS u32x2*)(size_t)(ev + w * 512u) = pk4(hv0);
        if (w == 0) *(LAS u32x2*)(size_t)(ev + 2048u) = pk4(hvx);
        prog_set(f_own, 11);
      }
    }
    const int par = li & 1;
    const bool cura = (4 * g + par) <= li, curb = (4 * g + par + 2) <= li;
    const unsigned hm = par ? 0xffff0000u : 0x0000ffffu;
    const unsigned ma0 = cura ? 0u : hm, ma1 = cura ? hm : 0u, mb0 = curb ? 0u : hm, mb1 = curb ? hm : 0u;
    const float SC = 0.22360679774997896f * 1.4426950408889634f, NEG = -1e9f * 1.4426950408889634f;
    const bool first = (rt == 0) && (w == 0);
    unsigned mq[4], mv[4];
#pragma unroll
    for (int i = 0; i < 4; ++i) { mq[i] = (g == (i ^ w)) ? 0xffffffffu : 0u; mv[i] = (q == (i ^ w)) ? 0xffffffffu : 0u; }
    auto attend = [&](const int bk, const u32x4 (&Kprev)[4], const u32x2 (&Vprev)[5]) {
      const bool fm = first && bk == 0;
      const float sca = (fm && !cura) ? 0.f : SC, bia = (fm && !cura) ? NEG : 0.f;
      const float scb = (fm && !curb) ? 0.f : SC, bib = (fm && !curb) ? NEG : 0.f;
      const u32x2 Qx = pk4(acc[4][bk]);
      f32x4 s0[4], s1[4];
#pragma unroll
      for (int i = 0; i < 4; ++i) {
        const u32x2 Qm = pk4(acc[i][bk]);
        const u32x4 Qf = {Qm[0], Qm[1], Qx[0] & mq[i], Qx[1] & mq[i]};
        s0[i] = mfmau(Kprev[i], Qf, f32x4{}); s1[i] = mfmau(Kt[i][bk], Qf, f32x4{});
      }
      u32x4 Vx[4];
#pragma unroll
      for (int i = 0; i < 4; ++i) Vx[i] = u32x4{Vprev[4][0] & mv[i], Vprev[4][1] & mv[i], Vp[4][bk][0] & mv[i], Vp[4][bk][1] & mv[i]};
      float la[4], lb[4], mx[4];
#pragma unroll
      for (int i = 0; i < 4; ++i) {
        const float s0a = par ? s0[i][1] : s0[i][0], s0b = par ? s0[i][3] : s0[i][2];
        const float s1a = par ? s1[i][1] : s1[i][0], s1b = par ? s1[i][3] : s1[i][2];
        la[i] = (cura ? s1a : s0a) * sca + bia; lb[i] = (curb ? s1b : s0b) * scb + bib;
        mx[i] = vmax(la[i], lb[i]);
      }
      swap32(mx[0], mx[1]); swap32(mx[2], mx[3]);
      float m01 = vmax(mx[0], mx[1]), m23 = vmax(mx[2], mx[3]);
      swap16(m01, m23);
      float mA = vmax(m01, m23), mB = mA;
      swap16(mA, mB);
      mx[0] = mA; mx[1] = mA; mx[2] = mB; mx[3] = mB;
      swap32(mx[0], mx[1]); swap32(mx[2], mx[3]);
      float ea[4], eb[4], sm[4];
#pragma unroll
      for (int i = 0; i < 4; ++i) {
        ea[i] = __builtin_amdgcn_exp2f(la[i] - mx[i]); eb[i] = __builtin_amdgcn_exp2f(lb[i] - mx[i]);
        sm[i] = ea[i] + eb[i];
      }
      swap32(sm[0], sm[1]); swap32(sm[2], sm[3]);
      float t01 = sm[0] + sm[1], t23 = sm[2] + sm[3];
      swap16(t01, t23);
      float iA = __builtin_amdgcn_rcpf(t01 + t23), iB = iA;
      swap16(iA, iB);
      sm[0] = iA; sm[1] = iA; sm[2] = iB; sm[3] = iB;
      swap32(sm[0], sm[1]); swap32(sm[2], sm[3]);
      f32x4 om[4], ox = {};
#pragma unroll
      for (int i = 0; i < 4; ++i) {
        const float pa = ea[i] * sm[i], pb = eb[i] * sm[i];
        const unsigned ta = pk2(pa, pa), tb = pk2(pb, pb);
        const u32x4 P = {ta & ma0, tb & mb0, ta & ma1, tb & mb1};
        om[i] = mfmau(u32x4{Vprev[i][0], Vprev[i][1], Vp[i][bk][0], Vp[i][bk][1]}, P, f32x4{});
        ox = mfmau(Vx[i], P, ox);
      }
      const unsigned zrow = vz + (unsigned)((rt * 8 + 2 * w + bk) * KS2) * 1024u;
      const bool sw = w & 1;
#pragma unroll
      for (int i = 0; i < 4; i += 2) {
        const u32x2 o0 = pk4(om[i]), o1 = pk4(om[i + 1]);
        const u32x4 v = {sw ? o1[0] : o0[0], sw ? o1[1] : o0[1], sw ? o0[0] : o1[0], sw ? o0[1] : o1[1]};
        __builtin_amdgcn_raw_buffer_store_b128(v, sz, zrow + (unsigned)(2 * G + ((i ^ w) >> 1)) * 1024u, 0, 16);
      }
      __builtin_amdgcn_raw_buffer_store_b64(pk4(ox), sz, zrow + (unsigned)(16 + (G >> 1)) * 1024u + 8u * (G & 1), 0, 16);
    };
    {
      const u32x4 Kp0[4] = {Kt[0][0], Kt[1][0], Kt[2][0], Kt[3][0]};
      const u32x2 Vp0[5] = {Vp[0][0], Vp[1][0], Vp[2][0], Vp[3][0], Vp[4][0]};
      attend(1, Kp0, Vp0);
    }
    prog_wait(f_cmp, prog_read(f_cmp), 11);
    u32x4 Kh[4]; u32x2 Vh[5];
#pragma unroll
    for (int i = 0; i < 4; ++i) {
      Kh[i] = *(LAS u32x4*)(size_t)(lds0 + lane * 16u + w * 8192u + (i ^ w) * 1024u);
      Vh[i] = *(LAS u32x2*)(size_t)(lds0 + 4096u + lane * 8u + w * 8192u + (i ^ w) * 512u);
    }
    Vh[4] = *(LAS u32x2*)(size_t)(lds0 + 4096u + lane * 8u + w * 8192u + 2048u);
    prog_set(f_own, 12);
    attend(0, Kh, Vh);
    STAMP(13);
    publish(p, lds0, lane, fidx, 3);
    STAMP(14); STAMP_RT(56);
    int cj, blk0;
    if (xi < 6) { cj = xi >> 1; blk0 = 24 + 4 * (xi & 1); } else { const int t = xi - 6; cj = t < 5 ? 3 : 4; blk0 = 12 + 4 * (t < 5 ? t : t - 5); }
    compute2<4>(p, lds0, lane, w, (xcd >> 1) * 32 + blk0, 5 * ch + cj, f_own DIAG_PASS);
  } else {
    const int wr = w >> 1, wc = w & 1;
    const unsigned f = 2 * (g & 1) + (q >> 1);
    unsigned wa[6];
#pragma unroll
    for (int n = 0; n < 6; ++n) wa[n] = lds0 + lrow * 384u + 32u * ((6 * wc + n) ^ f) + 8u * pq;
    unsigned xa[2];
#pragma unroll
    for (int ks = 0; ks < 2; ++ks) xa[ks] = lds0 + M_WB + (64 * wr + li) * 128u + 16u * ((4 * ks + g) ^ (lane & 7));
    f32x4 acc[6][4];
    bf16x8 wn[3], xn[4];
    unsigned wc_[6], xc_[2];
#define M_ADR(u, t) (wc_[(u) % 6] + (32 * ((u) / 6) + 8 * (t)) * 384u)
#pragma unroll
    for (int i = 0; i < 6; ++i) { wc_[i] = wa[i]; OPAQUE(wc_[i]); }
#pragma unroll
    for (int i = 0; i < 2; ++i) { xc_[i] = xa[i]; OPAQUE(xc_[i]); }
    prog_wait_sleep(f_oth, 1);
    STAMP(1);
    wn[0] = lds_tr8(M_ADR(0, 0), M_ADR(0, 1));
#pragma unroll
    for (int i = 0; i < 4; ++i) xn[i] = lds_b128(xc_[0] + 2048u * i);
    wn[1] = lds_tr8(M_ADR(1, 0), M_ADR(1, 1)); wn[2] = lds_tr8(M_ADR(2, 0), M_ADR(2, 1));
#pragma unroll
    for (int n = 0; n < 6; ++n) {
      const f32x4 bi = bf4_to_f32(*(LAS u32x2*)(size_t)(lds0 + L_BIAS + (96 * wc + 16 * n + 4 * g) * 2));
#pragma unroll
      for (int m = 0; m < 4; ++m) acc[n][m] = bi;
    }
    SB();
    unsigned sbn = M_STAGE;
    for (int st = 0; st < 10; ++st) {
      bf16x8 wf[12], xf[2][4];
      unsigned seen = 0;
#pragma unroll
      for (int i = 0; i < 3; ++i) wf[i] = wn[i];
#pragma unroll
      for (int i = 0; i < 4; ++i) xf[0][i] = xn[i];
#pragma unroll
      for (int u = 0; u < 12; ++u) {
        const int ks = u / 6, n = u % 6, tu = (u + 3) % 12;
        if (u == 8) {
          prog_wait(f_oth, seen, st < 9 ? st + 2 : 0);
          STAMPL(2 + st);
          SB();
        }
        acc[n][0] = mfma(wf[u], xf[ks][0], acc[n][0]);
        SB();
        const bf16x4 lo = lds_tr4(M_ADR(tu, 0));
        SB();
        acc[n][1] = mfma(wf[u], xf[ks][1], acc[n][1]);
        SB();
        const bf16x4 hi = lds_tr4(M_ADR(tu, 1));
        if (u < 9) wf[u + 3] = cat8(lo, hi); else wn[u - 9] = cat8(lo, hi);
        SB();
        acc[n][2] = mfma(wf[u], xf[ks][2], acc[n][2]);
        SB();
        if (u < 4) xf[1][u] = lds_b128(xc_[1] + 2048u * u);
        if (u == 5) seen = prog_read(f_oth);
        if (u >= 8) xn[u - 8] = lds_b128(xc_[0] + 2048u * (u - 8));
        SB();
        acc[n][3] = mfma(wf[u], xf[ks][3], acc[n][3]);
        SB();
        if (u >= 3 && u < 9) { wc_[u - 3] = wa[u - 3] + sbn; OPAQUE(wc_[u - 3]); }
        if (u == 1) { xc_[0] = xa[0] + sbn; OPAQUE(xc_[0]); }
        if (u == 9) { xc_[1] = xa[1] + sbn; OPAQUE(xc_[1]); }
        if (u == 8) prog_set(f_own, st + 1);
        if (u == 11) sbn = sbn == 2 * M_STAGE ? 0u : sbn + M_STAGE;
        SB();
      }
    }
    STAMP(12);
    prog_set(f_own, 12);
    const float C1 = -2.f * 0.7978845608028654f * 1.4426950408889634f, C2 = C1 * 0.044715f;
#pragma unroll
    for (int m = 0; m < 4; ++m) {
      const unsigned zrow = vz + (unsigned)((rt * 8 + 4 * wr + m) * KS2 + 20 + 6 * G + 3 * wc) * 1024u;
#pragma unroll
      for (int j = 0; j < 3; ++j) {
        u32x4 o;
#pragma unroll
        for (int e = 0; e < 2; ++e) {
#pragma unroll
          for (int k = 0; k < 2; ++k) {
            const f32x2 x = {acc[2 * j + e][m][2 * k], acc[2 * j + e][m][2 * k + 1]};
            const f32x2 t = x * (x * x * C2 + C1);
            f32x2 d = {__builtin_amdgcn_exp2f(t[0]), __builtin_amdgcn_exp2f(t[1])};
            d = d + 1.f;
            f32x2 r = {__builtin_amdgcn_rcpf(d[0]), __builtin_amdgcn_rcpf(d[1])};
            r = r * x;
            o[2 * e + k] = pk2(r[0], r[1]);
          }
        }
        __builtin_amdgcn_raw_buffer_store_b128(o, sz, zrow + 1024u * j, 0, 16);
      }
    }
    STAMP(13);
    publish(p, lds0, lane, fidx, 4);
    STAMP(14); STAMP_RT(56);
    int cj, blk0;
    if (xi < 12) { cj = xi >> 2; blk0 = 6 * (xi & 3); } else { cj = 3 + ((xi - 12) >> 1); blk0 = 6 * (xi & 1); }
    compute2<6>(p, lds0, lane, w, (xcd >> 1) * 32 + blk0, 5 * ch + cj, f_own DIAG_PASS);
  }
  STAMP(59);
  STAMP_OUT();
}

extern "C" void kernel_launch(void* const* d_in, const int* in_sizes, int n_in, void* d_out, int out_size, void* d_ws,
                              size_t ws_size, hipStream_t stream) {
  (void)in_sizes; (void)n_in; (void)out_size; (void)ws_size;
  (void)hipFuncSetAttribute((const void*)fused, hipFuncAttributeMaxDynamicSharedMemorySize, L_TOTAL);
  Args a{d_in[0], d_in[1], d_in[4], d_in[5], d_in[8], d_in[2], d_in[6], d_in[7], d_out, (char*)d_ws};
  fused<<<256, 512, L_TOTAL, stream>>>(a);
}
```

```cpp
#define AMP_BLK 0
#define AMP_A 61
#define AMP_B 61
#include <hip/hip_runtime.h>
#include <hip/hip_bf16.h>
#include <stdint.h>
#include <type_traits>

typedef __attribute__((ext_vector_type(8))) __bf16 bf16x8;
typedef __attribute__((ext_vector_type(4))) __bf16 bf16x4;
typedef __attribute__((ext_vector_type(2))) __bf16 bf16x2;
typedef __attribute__((ext_vector_type(4))) float f32x4;
typedef __attribute__((ext_vector_type(2))) float f32x2;
typedef __attribute__((ext_vector_type(4))) unsigned u32x4;
typedef __attribute__((ext_vector_type(2))) unsigned u32x2;
typedef __amdgpu_buffer_rsrc_t srd_t;
typedef __attribute__((address_space(1))) unsigned long long gu64;
typedef __attribute__((address_space(1))) unsigned gu32;
#define LAS __attribute__((address_space(3)))
#define DI __device__ __forceinline__
#define RLX_AGENT __ATOMIC_RELAXED, __HIP_MEMORY_SCOPE_AGENT
#define RLX_WG __ATOMIC_RELAXED, __HIP_MEMORY_SCOPE_WORKGROUP
#define SB() __builtin_amdgcn_sched_barrier(0)
#define OPAQUE(x) asm volatile("" : "+v"(x))
#define FENCE() asm volatile("" ::: "memory")
#define WAITV0() asm volatile("s_waitcnt vmcnt(0)" ::: "memory")
#define LGKM_BAR() asm volatile("s_waitcnt lgkmcnt(0)\n\ts_barrier" ::: "memory")

#define PFMODE 3
#define PFS 4
#define ROLEX 0
#define WPACE 0
#define ZSPREAD 1
#define WEARLY 0
#define W0AT 10
#define PF2ST 0

constexpr int S_ = 2048, D_ = 640, F_ = 1536;
constexpr int KS2 = 68;
constexpr unsigned Z_BYTES = 128u * KS2 * 1024u;
constexpr unsigned WS_GATE = Z_BYTES, WS_FLAGS = WS_GATE + 8192, WS_EPOCH = WS_FLAGS + 2048;
constexpr unsigned SPIN_CAP = 1u << 18;
constexpr unsigned A_WB = 30720, A_STAGE = 49152;
constexpr unsigned M_WB = 24576, M_STAGE = 40960;
constexpr unsigned L_MISC = 3 * A_STAGE;
constexpr unsigned L_WROW = L_MISC + 2048;
constexpr unsigned L_SCR = L_WROW + 1024, L_GATE = L_SCR + 256, L_B2 = L_GATE + 1024, L_BIAS = L_B2 + 1024, L_GW = L_BIAS + 1024;
constexpr unsigned L_WORDS = L_GW + 2048, L_TOTAL = L_WORDS + 64;

struct Args { const void* x; const void* wqkv; const void* w1; const void* b1; const void* gw; const void* wo; const void* w2; const void* b2; void* out; char* ws; };

#define STAMP_DECL
#define STAMP(slot)
#define STAMPL(slot)
#define STAMP_RT(slot)
#define STAMP_OUT()
#define DIAG_ARG
#define DIAG_PASS

#undef STAMP_DECL
#undef STAMP_RT
#undef STAMP_OUT
#undef DIAG_ARG
#undef DIAG_PASS
#define DIAG_ARG , unsigned long long& amp_a, unsigned long long& amp_b
#define DIAG_PASS , amp_a, amp_b
#define STAMP_DECL unsigned long long amp_a = 0, amp_b = 0;
#define STAMP_RT(slot) do{ if ((slot) == AMP_A || (slot) == AMP_B) { unsigned long long _c; asm volatile("s_memrealtime %0\n\ts_waitcnt lgkmcnt(0)" : "=s"(_c) :: "memory"); if ((slot) == AMP_A) amp_a = _c; if ((slot) == AMP_B) amp_b = _c; } }while(0)
#define STAMP_OUT() do{ WAITV0(); STAMP_RT(63); if (blockIdx.x == AMP_BLK && wid == 4) { unsigned d = (unsigned)(amp_b - amp_a) + 100u; d = d > 4000u ? 4000u : d; \
      unsigned long long t0 = __builtin_amdgcn_s_memrealtime(); while (__builtin_amdgcn_s_memrealtime() - t0 < 10ull * d) {} } }while(0)
DI srd_t make_srd(const void* p, unsigned bytes) { return __builtin_amdgcn_make_buffer_rsrc((void*)p, (short)0, (int)bytes, 0x00020000); }
DI void dma16(unsigned voff, srd_t srd, unsigned soff, unsigned lds) {
  asm volatile("s_mov_b32 m0, %0\n\ts_nop 0\n\tbuffer_load_dwordx4 %1, %2, %3 offen lds" :: "s"(lds), "v"(voff), "s"(srd), "s"(soff) : "memory");
}
template <unsigned HI> DI void dma16m(unsigned voff, srd_t srd, unsigned soff, unsigned lds) {
  asm volatile("s_mov_b32 m0, %0\n\ts_mov_b32 exec_hi, %4\n\tbuffer_load_dwordx4 %1, %2, %3 offen lds\n\ts_mov_b32 exec_hi, -1"
               :: "s"(lds), "v"(voff), "s"(srd), "s"(soff), "i"(HI) : "memory");
}
DI void dma16c(unsigned voff, srd_t srd, unsigned soff, unsigned lds) {
  asm volatile("s_mov_b32 m0, %0\n\ts_nop 0\n\tbuffer_load_dwordx4 %1, %2, %3 offen sc1 lds" :: "s"(lds), "v"(voff), "s"(srd), "s"(soff) : "memory");
}
DI void dma16c1(unsigned voff, srd_t srd, unsigned soff, unsigned lds) {
  asm volatile("s_mov_b32 m0, %0\n\ts_mov_b64 exec, 1\n\tbuffer_load_dwordx4 %1, %2, %3 offen sc1 lds\n\ts_mov_b64 exec, -1"
               :: "s"(lds), "v"(voff), "s"(srd), "s"(soff) : "memory");
}
__device__ const unsigned g_seq[16] = {0, 1, 2, 3, 4, 5, 6, 7, 8, 9, 10, 11, 12, 13, 14, 15};
DI void dma4(unsigned voff, srd_t srd, unsigned soff, unsigned lds) {
  asm volatile("s_mov_b32 m0, %0\n\ts_mov_b64 exec, 1\n\tbuffer_load_dword %1, %2, %3 offen lds\n\ts_mov_b64 exec, -1"
               :: "s"(lds), "v"(voff), "s"(srd), "s"(soff) : "memory");
}
DI void touch(const char* addr, unsigned lds_scratch) {
  asm volatile("s_mov_b32 m0, %0\n\ts_nop 0\n\tglobal_load_lds_dword %1, off" :: "s"(lds_scratch), "v"(addr) : "memory");
}
DI bf16x4 lds_tr4(unsigned a) { return __builtin_amdgcn_ds_read_tr16_b64_v4bf16((LAS bf16x4*)(size_t)a); }
DI bf16x8 cat8(bf16x4 lo, bf16x4 hi) { return __builtin_shufflevector(lo, hi, 0, 1, 2, 3, 4, 5, 6, 7); }
DI bf16x8 lds_tr8(unsigned a0, unsigned a1) { return cat8(lds_tr4(a0), lds_tr4(a1)); }
DI bf16x8 lds_b128(unsigned a) { return *(LAS bf16x8*)(size_t)a; }
DI f32x4 mfma(bf16x8 a, bf16x8 b, f32x4 c) { return __builtin_amdgcn_mfma_f32_16x16x32_bf16(a, b, c, 0, 0, 0); }
DI f32x4 mfmau(u32x4 a, u32x4 b, f32x4 c) { return mfma(__builtin_bit_cast(bf16x8, a), __builtin_bit_cast(bf16x8, b), c); }
#define MF(c, a, b) asm volatile("v_mfma_f32_16x16x32_bf16 %0, %1, %2, %0" : "+v"(c) : "v"(a), "v"(b))
#define MF0(c, a, b) asm volatile("v_mfma_f32_16x16x32_bf16 %0, %1, %2, 0" : "=&v"(c) : "v"(a), "v"(b))
DI unsigned pk2(float a, float b) { bf16x2 v = {(__bf16)a, (__bf16)b}; return __builtin_bit_cast(unsigned, v); }
DI u32x2 pk4(f32x4 v) { return u32x2{pk2(v[0], v[1]), pk2(v[2], v[3])}; }
#define vmax(a, b) __builtin_amdgcn_fmed3f(a, b, pinf)
DI f32x4 bf4_to_f32(u32x2 v) {
  return f32x4{__uint_as_float(v[0] << 16), __uint_as_float(v[0] & 0xffff0000u), __uint_as_float(v[1] << 16), __uint_as_float(v[1] & 0xffff0000u)};
}
DI void swap32(float& a, float& b) {
  auto r = __builtin_amdgcn_permlane32_swap(__float_as_uint(a), __float_as_uint(b), false, false);
  a = __uint_as_float(r[0]); b = __uint_as_float(r[1]);
}
DI void swap16(float& a, float& b) {
  auto r = __builtin_amdgcn_permlane16_swap(__float_as_uint(a), __float_as_uint(b), false, false);
  a = __uint_as_float(r[0]); b = __uint_as_float(r[1]);
}
template <int N, class F> DI void static_for(F&& f) {
  if constexpr (N > 0) { static_for<N - 1>(f); f(std::integral_constant<int, N - 1>{}); }
}

DI void prog_set(unsigned row_addr, unsigned v) { FENCE(); __hip_atomic_store((LAS unsigned*)(size_t)row_addr, v, RLX_WG); FENCE(); }
DI unsigned prog_read(unsigned addr) { return __hip_atomic_load((LAS unsigned*)(size_t)addr, RLX_WG); }
DI void prog_wait(unsigned addr, unsigned seen, unsigned need) {
  if (__builtin_expect(__builtin_amdgcn_ballot_w64(seen < need) != 0, 0)) {
    unsigned long long m;
    do {
      unsigned v;
      asm volatile("ds_read_b32 %0, %1\n\ts_waitcnt lgkmcnt(0)" : "=v"(v) : "v"(addr) : "memory");
      m = __builtin_amdgcn_ballot_w64(v < need);
    } while (m != 0);
  }
  FENCE();
}
DI void prog_wait_sleep(unsigned addr, unsigned need) {
  while (__builtin_amdgcn_ballot_w64(prog_read(addr) < need) != 0) __builtin_amdgcn_s_sleep(1);
  FENCE();
}
template <unsigned NEED> DI void prog_wait_inl(unsigned addr, unsigned seen) {
  asm volatile("v_cmp_gt_u32 vcc, %2, %0\n\ts_cbranch_vccz 2%=f\n1%=:\n\ts_sleep 1\n\tds_read_b32 %0, %1\n\ts_waitcnt lgkmcnt(0)\n\t"
               "v_cmp_gt_u32 vcc, %2, %0\n\ts_cbranch_vccnz 1%=b\n2%=:"
               : "+v"(seen) : "v"(addr), "i"(NEED) : "vcc", "memory");
}
DI unsigned swz23(unsigned k) { return (k & ~12u) | ((k & 4u) << 1) | ((k & 8u) >> 1); }

DI unsigned long long get_epoch(const unsigned lds0) { return *(LAS unsigned long long*)(size_t)(lds0 + L_WORDS + 16); }
DI unsigned long long get_tag(const unsigned lds0) { return (get_epoch(lds0) + 1) * 0x9E3779B97F4A7C15ull; }
template <int YOUNGER = 0>
DI void publish(const Args& p, const unsigned lds0, const int lane, const int fidx, const unsigned last) {
  asm volatile("s_waitcnt vmcnt(%0)" :: "i"(YOUNGER) : "memory");
  if (lane == 0) {
    const unsigned old = __hip_atomic_fetch_add((LAS unsigned*)(size_t)(lds0 + L_WORDS), 1u, RLX_WG);
    if (old == last) __hip_atomic_store((gu64*)(p.ws + WS_FLAGS) + fidx, get_tag(lds0), RLX_AGENT);
  }
  FENCE();
}
DI void prefetch_w2(const Args& p, const unsigned lds0, const int lane, const int wv128, const int ch) {
#pragma unroll
  for (int j = 0; j < 2; ++j) {
    int L = (j * 128 + wv128) * 43 + lane;
    L = (L < 10880 && lane < 43) ? L : 0;
    const int r = L / 5, cl = L % 5;
    touch((r < 1536 ? (const char*)p.w2 + r * 1280 : (const char*)p.wo + (r - 1536) * 1280) + ch * 640 + cl * 128, lds0 + L_SCR);
  }
}

constexpr int NW2 = 18;
template <int NB> constexpr int zdepth() { return NB == 6 ? 8 : 12; }

template <int NB>
DI void loader2(const Args& p, const unsigned lds0, const int lane, const int lw, const int tb0, const int cn, const unsigned f_cmp, const int fidx, const bool gw_wave DIAG_ARG) {
  const unsigned f_oth = (lane & 4) ? lds0 + L_WORDS + 8 : f_cmp;
  const srd_t so = make_srd(p.wo, D_ * D_ * 2);
  const srd_t s2 = make_srd(p.w2, F_ * D_ * 2);
  const unsigned rho = 8 * lw + (lane >> 3), t = rho >> 4, gg = (rho >> 2) & 3, qq = rho & 3;
  const unsigned cpos = lane & 7, sch = (((cpos >> 1) ^ ((rho >> 1) & 3)) << 1) | (cpos & 1);
  const unsigned cb = 128u * cn + 16u * sch;
  const unsigned vA = (20 * t + (rho & 15)) * 1280u + cb;
  const unsigned vB = (80 * t + 20 * gg + 16 + qq) * 1280u + cb;
  const unsigned vC = rho * 1280u + cb;
  const unsigned vz = lane * 16u;
  const unsigned wl = lds0 + 1024u * lw;
  const unsigned f_w = lds0 + L_WROW + lw * 256u + lane * 4u;
  u32x4 wreg[2 * (34 - NW2)];
  auto load_wreg = [&]() {
#pragma unroll
    for (int k = 2 * NW2; k < 68; ++k) {
      if (WPACE && (k & 1) == 0) { SB(); __builtin_amdgcn_s_sleep(WPACE); SB(); }
      wreg[k - 2 * NW2] = k < 48 ? __builtin_amdgcn_raw_buffer_load_b128(s2, vC, 40960 * k, 0)
                        : (k < 64 ? __builtin_amdgcn_raw_buffer_load_b128(so, vA, 51200 * (k - 48), 0)
                                  : __builtin_amdgcn_raw_buffer_load_b128(so, vB, 204800 * (k - 64), 0));
    }
  };
  prog_wait_sleep(f_oth, W0AT);
  for (int k = 0; k < 24; ++k) { if (WPACE && (k & 1) == 0) __builtin_amdgcn_s_sleep(WPACE); dma16(vC, s2, 40960u * k, wl + 49152u + 4096u * k); }
  if (gw_wave) publish<24>(p, lds0, lane, fidx, 4);
  WAITV0();
  prog_set(f_w, 10 + 12);
  STAMP_RT(45);
  const unsigned long long tag = get_tag(lds0);
  const gu64* fl = (const gu64*)(p.ws + WS_FLAGS) + (((lane & 16) ? (tb0 + NB - 1) : tb0) >> 3) * 16 + (lane & 15);
  int last = 0;
  auto poll = [&]() {
    const unsigned long long v = __hip_atomic_load(fl, RLX_AGENT);
    const unsigned long long m = __builtin_amdgcn_ballot_w64(v == tag);
    const int n = __builtin_ctz(~((unsigned)m & (unsigned)(m >> 16) & 0xffffu));
    const int pub = n == 16 ? 63 : (n <= 8 ? 3 * n : 16 + n) + 11 - zdepth<NB>();
    if (pub > last) {
      last = pub;
      FENCE();
      __hip_atomic_store((LAS unsigned*)(size_t)(lds0 + L_WORDS + 4), (unsigned)pub, RLX_WG);
      FENCE();
    }
  };
  while (__builtin_amdgcn_readfirstlane(prog_read(lds0 + L_WORDS)) < (NB == 4 ? 4u : 5u)) {
    if (lw == 0 && last < 35 - zdepth<NB>()) poll(); else __builtin_amdgcn_s_sleep(2);
  }
  FENCE();
  if (lw == 0) {
#pragma nounroll
    for (unsigned spins = 0; spins < SPIN_CAP && last < 11; ++spins) poll();
  }
  for (int k = 24; k < 36; ++k) dma16(vC, s2, 40960u * k, wl + 4096u * (k - 24));
  load_wreg();
  if (lw == 1) dma16(vz, make_srd(p.b2, D_ * 2), cn * 128u, lds0 + L_B2);
  STAMP(16); STAMP_RT(56);
  if (lw == 0) {
    poll();
    prog_set(f_w, 10 + NW2);
#pragma nounroll
    for (unsigned spins = 0; spins < SPIN_CAP && last < 63; ++spins) { __builtin_amdgcn_s_sleep(1); poll(); }
    FENCE();
    __hip_atomic_store((LAS unsigned*)(size_t)(lds0 + L_WORDS + 4), 63u, RLX_WG);
    dma16c(vz, make_srd(p.ws + WS_GATE, S_ * 4), tb0 * 64u, lds0 + L_GATE);
  }
  __builtin_amdgcn_s_waitcnt(0x0F70);
  prog_set(f_w, 10 + NW2);
  STAMP(17); STAMP_RT(57);
  const unsigned wv = wl + vz;
  unsigned seen = prog_read(f_cmp);
  static_for<34 - NW2>([&](auto jc) {
    constexpr int j = decltype(jc)::value;
    prog_wait_inl<13 + j>(f_cmp, seen);
    seen = prog_read(f_cmp);
    *(LAS u32x4*)(size_t)(wv + ((j + 6) % NW2) * 8192u) = wreg[2 * j];
    *(LAS u32x4*)(size_t)(wv + ((j + 6) % NW2) * 8192u + 4096u) = wreg[2 * j + 1];
    prog_set(f_w, 11 + NW2 + j);
  });
  if (blockIdx.x == 0 && lw == 0) {
    const gu64* fa = (const gu64*)(p.ws + WS_FLAGS) + lane;
#pragma nounroll
    for (unsigned spins = 0; spins < SPIN_CAP; ++spins) {
      bool ok = true;
#pragma unroll
      for (int j = 0; j < 4; ++j) ok &= __hip_atomic_load(fa + 64 * j, RLX_AGENT) == tag;
      if (__builtin_amdgcn_ballot_w64(!ok) == 0) break;
      __builtin_amdgcn_s_sleep(2);
    }
    if (lane == 0) __hip_atomic_store((gu64*)(p.ws + WS_EPOCH), get_epoch(lds0) + 1, RLX_AGENT);
  }
}

template <int NB>
DI void compute2(const Args& p, const unsigned lds0, const int lane, const int w, const int tb0, const int cn, const unsigned f_own, const int fidx DIAG_ARG) {
  constexpr int MB = NB / 2, ZD = zdepth<NB>();
  const unsigned f_oth = (lane & 4) ? lds0 + L_WORDS + 4 : lds0 + L_WROW + (lane & 3) * 260u;
  const unsigned f_cmp = lds0 + L_MISC + 1024u + (lane & 3) * 260u;
  const int wr = w >> 1, e = w & 1;
  const int g = lane >> 4, q = (lane & 15) >> 2, pq = lane & 3, li = lane & 15;
  const srd_t sz = make_srd(p.ws, Z_BYTES);
  unsigned zv[MB];
#pragma unroll
  for (int m = 0; m < MB; ++m) zv[m] = (unsigned)(((tb0 + MB * wr + m) * KS2 + e) * 1024) + lane * 16u;
  unsigned wa[3][4];
  {
    const unsigned r = 4 * g + q;
#pragma unroll
    for (int n = 0; n < 4; ++n) {
      wa[0][n] = lds0 + e * 4096u + r * 128u + 32u * (n ^ ((r >> 1) & 3)) + 8u * pq;
      wa[1][n] = wa[0][n] + 49152u; wa[2][n] = wa[0][n] + 98304u;
      OPAQUE(wa[0][n]); OPAQUE(wa[1][n]); OPAQUE(wa[2][n]);
    }
  }
  f32x4 acc[2][4][MB];
  u32x4 zf[ZD][MB];
  bf16x8 wf[2][4];
#define C2_ZLD(s, m) __builtin_amdgcn_raw_buffer_load_b128(sz, zv[m], ((s) < 24 ? 20 + 2 * (s) : 2 * (s) - 48) * 1024, 16)
#define C2_WAD(s, n) (wa[(((s) + 6) % NW2) / 6][n] + ((((s) + 6) % NW2) % 6) * 8192u)
  const bool early = __builtin_amdgcn_ballot_w64(prog_read(f_oth) < 11u) == 0;
  FENCE();
  if (!early) {
    publish(p, lds0, lane, fidx, NB == 4 ? 3 : 4);
    STAMP(14); STAMP_RT(56);
    prog_wait_sleep(f_oth, 11);
  }
  STAMP(20); STAMP_RT(57);
#pragma unroll
  for (int s = 0; s < ZD; ++s) {
#pragma unroll
    for (int m = 0; m < MB; ++m) zf[s][m] = C2_ZLD(s, m);
    SB();
  }
  if (early) { publish<ZD * MB>(p, lds0, lane, fidx, NB == 4 ? 3 : 4); STAMP_RT(56); }
  unsigned seen = prog_read(f_oth);
  SB();
#pragma unroll
  for (int n = 0; n < 4; ++n) wf[0][n] = lds_tr8(C2_WAD(0, n), C2_WAD(0, n) + 2048u);
  prog_set(f_own, 13);
  unsigned one = 1; OPAQUE(one);
  static_for<34>([&](auto sc) {
    constexpr int st = decltype(sc)::value, pp = st & 1, h = st < 24 ? 1 : 0, sn = st < 33 ? st + 1 : 33;
    if constexpr (st < 33) prog_wait_inl<st + 12>(f_oth, seen);
    STAMPL(21 + st);
    if (st == 1) STAMP_RT(47);
    if (st == 12) STAMP_RT(48);
    if (st == 24) STAMP_RT(49);
    SB();
    bf16x4 lo[4], hi[4];
    u32x4 zc[MB];
#pragma unroll
    for (int m = 0; m < MB; ++m) zc[m] = zf[st % ZD][m];
#define C2_LO(n) lo[n] = lds_tr4(C2_WAD(sn, n))
#define C2_HI(n) do { hi[n] = lds_tr4(C2_WAD(sn, n) + 2048u); wf[pp ^ 1][n] = cat8(lo[n], hi[n]); } while (0)
#pragma unroll
    for (int k = 0; k < 4 * MB; ++k) {
      const int n = k / MB, m = k % MB;
      if (st == 0 || st == 24) MF0(acc[h][n][m], wf[pp][n], zc[m]); else MF(acc[h][n][m], wf[pp][n], zc[m]);
      SB();
      if (st < 33) {
        if (k == 0) { C2_LO(0); seen = prog_read(f_oth); }
        if (k == 1) C2_HI(0);
        if (k == 2) C2_LO(1);
        if (k == 3) C2_HI(1);
        if (k == 4) C2_LO(2);
        if (k == 5) C2_HI(2);
        if (k == 6) C2_LO(3);
        if (k == 7) { C2_HI(3); FENCE(); __hip_atomic_fetch_add((LAS unsigned*)(size_t)f_own, one, RLX_WG); FENCE(); }
      }
      if (k % 4 == 2 && st > 0 && st + ZD - 1 < 34) zf[(st - 1) % ZD][k / 4] = C2_ZLD(st + ZD - 1, k / 4);
      SB();
    }
  });
  float gt[MB];
#pragma unroll
  for (int m = 0; m < MB; ++m) gt[m] = *(LAS float*)(size_t)(lds0 + L_GATE + ((MB * wr + m) * 16 + li) * 4);
  const unsigned seen2 = prog_read(f_cmp);
  asm volatile("s_nop 7\n\ts_nop 7" ::: "memory");
  SB();
  STAMP(58); STAMP_RT(50);
  prog_wait(f_cmp, seen2, 13 + 25);
  constexpr unsigned HALF = NB * 16 * 272;
  const unsigned xw = lds0 + 49152u + e * HALF + (wr * MB * 16 + li) * 272u + g * 16u;
#pragma unroll
  for (int m = 0; m < MB; ++m)
#pragma unroll
    for (int n = 0; n < 4; ++n) *(LAS f32x4*)(size_t)(xw + m * 16 * 272u + n * 64u) = acc[0][n][m] * gt[m] + acc[1][n][m] * (1.f - gt[m]);
  prog_set(f_own, 50);
  const int r0 = w * (NB * 4) + (lane >> 3);
  const u32x4 bv = *(LAS u32x4*)(size_t)(lds0 + L_B2 + (lane & 7) * 16u);
  float g2[NB / 2];
#pragma unroll
  for (int j = 0; j < NB / 2; ++j) g2[j] = 1.f - *(LAS float*)(size_t)(lds0 + L_GATE + (r0 + 8 * j) * 4);
  prog_wait(f_cmp, prog_read(f_cmp), 50);
  const unsigned xr = lds0 + 49152u + r0 * 272u + (lane & 7) * 32u;
  char* ob = (char*)p.out + (size_t)(tb0 * 16 * D_ + 64 * cn) * 2;
  const unsigned vo = (r0 * D_ + (lane & 7) * 8) * 2;
  const f32x4 b0 = bf4_to_f32(u32x2{bv[0], bv[1]}), b1 = bf4_to_f32(u32x2{bv[2], bv[3]});
#pragma unroll
  for (int j = 0; j < NB / 2; ++j) {
    const unsigned a = xr + j * 8 * 272u;
    const f32x4 lo = *(LAS f32x4*)(size_t)a + *(LAS f32x4*)(size_t)(a + HALF) + b0 * g2[j];
    const f32x4 hi = *(LAS f32x4*)(size_t)(a + 16u) + *(LAS f32x4*)(size_t)(a + HALF + 16u) + b1 * g2[j];
    const u32x2 pl = pk4(lo), ph = pk4(hi);
    *(u32x4*)(ob + (size_t)(j * 8 * D_ * 2) + (size_t)vo) = u32x4{pl[0], pl[1], ph[0], ph[1]};
  }
}

__global__ __launch_bounds__(512, 2) void fused(Args p) {
  extern __shared__ __attribute__((aligned(16))) char shm[];
  const unsigned lds0 = (unsigned)(uintptr_t)(LAS char*)shm;
  const int lane = threadIdx.x & 63;
  const int wid = __builtin_amdgcn_readfirstlane(threadIdx.x >> 6) ^ ROLEX;
  const int b = blockIdx.x, xcd = b & 7, idx = b >> 3;
  const int rt = (xcd >> 1) * 4 + (idx & 3), c = idx >> 2, ch = xcd & 1;
  const bool is_attn = c < 4;
  const int G = 4 * ch + (c & 3);
  const int T0 = rt * 128;
  const int xi = idx & 15;
  const int fidx = rt * 16 + (is_attn ? 8 + G : G);
  const unsigned f_own = lds0 + L_MISC + wid * 256u + lane * 4u;
  const unsigned f_oth = lds0 + L_MISC + (wid < 4 ? 1024u : 0u) + (lane & 3) * 260u;
  prog_set(f_own, 0u);
  STAMP_DECL
  STAMP(0); STAMP_RT(61);

  if (wid < 4) {
    const int lw = wid;
    const srd_t sx = make_srd(p.x, S_ * D_ * 2);
    const srd_t sq = make_srd(g_seq, 64);
    const unsigned vzero = 0, f_row = lds0 + L_MISC + wid * 260u;
    const unsigned vx = (lane >> 3) * 1280u + 16u * ((lane & 7) ^ (lane >> 3));
    prog_set(lds0 + L_WROW + lw * 256u + lane * 4u, 0u);
    if (lw == 0 && lane == 0) *(LAS u32x4*)(size_t)(lds0 + L_WORDS) = u32x4{0u, 0u, is_attn ? 63u : 0u, 0u};
    if (is_attn) {
      const srd_t sw = make_srd(p.wqkv, D_ * 3 * D_ * 2);
      const unsigned r2 = lane >= 30 ? 1u : 0u, rem = lane - 30u * r2, sec = (rem >= 20 ? 2u : (rem >= 10 ? 1u : 0u));
      const unsigned vw = r2 * 3840u + sec * 1280u + 16u * (rem - 10u * sec);
      const int hr = 4 * lw + (lane >> 3);
      const int ht = T0 - 16 + hr;
      const unsigned vh = (unsigned)(ht < 0 ? 0 : ht) * 1280u + 16u * ((lane & 7) ^ (hr & 7));
      auto issue = [&](int st, unsigned lb) {
        const unsigned sow = st * 245760u + 160u * G, sox = st * 128u + (unsigned)T0 * 1280u;
        dma16m<0u>(vh, sx, st * 128u, lb + A_WB + 512u * lw);
#pragma unroll
        for (int i = 0; i < 4; ++i) { const unsigned n = 4 * lw + i; dma16(vx, sx, sox + n * 10240u, lb + A_WB + 2048u + 1024u * n); }
#pragma unroll
        for (int i = 0; i < 8; ++i) { const unsigned n = 8 * lw + i; dma16m<0x0fffffffu>(vw, sw, sow + swz23(2 * n) * 3840u, lb + 960u * n); }
      };
      STAMP(1);
      issue(0, lds0);
      if (lw == 0) dma16c1(vzero, make_srd(p.ws + WS_EPOCH, 16), 0u, lds0 + L_WORDS + 16);
      LGKM_BAR();
      dma4(vzero, sq, 4, f_row);
      unsigned lb = lds0 + A_STAGE;
      for (int st = 1; st < 10; ++st) {
        if (st >= 3) prog_wait_sleep(f_oth, st - 2);
        issue(st, lb);
        dma4(vzero, sq, 4 * st + 4, f_row);
        if (PF2ST > 0 && st == PF2ST) prefetch_w2(p, lds0, lane, idx * 4 + lw, ch);
        STAMPL(1 + st);
        lb = lb == lds0 + 2 * A_STAGE ? lds0 : lb + A_STAGE;
      }
      STAMP(15); STAMP_RT(44);
      int cj, blk0;
      if (xi < 6) { cj = xi >> 1; blk0 = 24 + 4 * (xi & 1); } else { const int t = xi - 6; cj = t < 5 ? 3 : 4; blk0 = 12 + 4 * (t < 5 ? t : t - 5); }
      loader2<4>(p, lds0, lane, lw, (xcd >> 1) * 32 + blk0, 5 * ch + cj, f_oth, fidx, false DIAG_PASS);
    } else {
      const srd_t sw = make_srd(p.w1, D_ * F_ * 2);
      unsigned vw[3];
#pragma unroll
      for (int i = 0; i < 3; ++i) {
        const unsigned cw = 64 * i + lane, j = (cw * 171u) >> 12, cp = cw - 24u * j;
        vw[i] = (8u * (j >> 2) + (j & 3)) * 3072u + 32u * ((cp >> 1) ^ ((j >> 1) & 3)) + 16u * (cp & 1);
      }
      auto issue = [&](int st, unsigned lb) {
        const unsigned sow = st * 196608u + 384u * G + (16u * (lw >> 1) + 4u * (lw & 1)) * 3072u, sox = st * 128u + (unsigned)T0 * 1280u;
#pragma unroll
        for (int i = 0; i < 4; ++i) { const unsigned n = 4 * lw + i; dma16(vx, sx, sox + n * 10240u, lb + M_WB + 1024u * n); }
#pragma unroll
        for (int i = 0; i < 3; ++i) dma16(vw[i], sw, sow, lb + 3072u * lw + 1024u * i);
#pragma unroll
        for (int i = 0; i < 3; ++i) dma16(vw[i], sw, sow + 32u * 3072u, lb + 3072u * (lw + 4) + 1024u * i);
      };
      STAMP(1);
      issue(0, lds0);
      if (lw == 0) dma16c1(vzero, make_srd(p.ws + WS_EPOCH, 16), 0u, lds0 + L_WORDS + 16);
      if (lw == 1) dma16(lane * 16u, make_srd(p.b1, F_ * 2), 384u * G, lds0 + L_BIAS);
      const bool gw_wave = lw == (G >> 1);
      if (gw_wave) { const srd_t sg = make_srd(p.gw, D_ * 2); dma16(lane * 16u, sg, 0u, lds0 + L_GW); dma16(lane * 16u, sg, 1024u, lds0 + L_GW + 1024u); }
      LGKM_BAR();
      dma4(vzero, sq, 4, f_row);
      const unsigned gxa = M_WB + 2048u * G + lane * 16u, gwa = lds0 + L_GW + 16u * ((lane & 7) ^ (lane >> 3));
      float ga0 = 0.f, ga1 = 0.f;
      auto gate_step = [&](const u32x4 x0, const u32x4 x1, const u32x4 gv) {
#pragma unroll
        for (int e = 0; e < 4; ++e) {
          const float gl = __uint_as_float(gv[e] << 16), gh = __uint_as_float(gv[e] & 0xffff0000u);
          ga0 += __uint_as_float(x0[e] << 16) * gl; ga0 += __uint_as_float(x0[e] & 0xffff0000u) * gh;
          ga1 += __uint_as_float(x1[e] << 16) * gl; ga1 += __uint_as_float(x1[e] & 0xffff0000u) * gh;
        }
      };
      unsigned lb = lds0 + M_STAGE;
      for (int st = 1; st < 10; ++st) {
        u32x4 gx0 = {}, gx1 = {}, gv = {};
        if (st >= 3) {
          prog_wait_sleep(f_oth, st - 2);
          if (gw_wave) {
            gx0 = *(LAS u32x4*)(size_t)(lb + gxa); gx1 = *(LAS u32x4*)(size_t)(lb + gxa + 1024u); gv = *(LAS u32x4*)(size_t)(gwa + 128u * (st - 3));
            asm volatile("s_waitcnt lgkmcnt(0)" : "+v"(gx0), "+v"(gx1), "+v"(gv) :: "memory");
          }
        }
        issue(st, lb);
        dma4(vzero, sq, 4 * st + 4, f_row);
        if (PF2ST > 0 && st == PF2ST) prefetch_w2(p, lds0, lane, idx * 4 + lw, ch);
        if (gw_wave) gate_step(gx0, gx1, gv);
        STAMPL(1 + st);
        lb = lb == lds0 + 2 * M_STAGE ? lds0 : lb + M_STAGE;
      }
      if (gw_wave) {
        __builtin_amdgcn_s_setprio(3);
        for (int st = 7; st < 10; ++st) {
          while (__builtin_amdgcn_readfirstlane(prog_read(f_row)) < (unsigned)(st + 1)) __builtin_amdgcn_s_sleep(1);
          FENCE();
          const u32x4 gx0 = *(LAS u32x4*)(size_t)(lb + gxa), gx1 = *(LAS u32x4*)(size_t)(lb + gxa + 1024u), gv = *(LAS u32x4*)(size_t)(gwa + 128u * st);
          FENCE();
          __hip_atomic_store((LAS unsigned*)(size_t)(lds0 + L_WORDS + 8), st < 9 ? (unsigned)(st + 1) : 63u, RLX_WG);
          FENCE();
          gate_step(gx0, gx1, gv);
          lb = lb == lds0 + 2 * M_STAGE ? lds0 : lb + M_STAGE;
        }
#pragma unroll
        for (int o = 4; o > 0; o >>= 1) { ga0 += __shfl_xor(ga0, o); ga1 += __shfl_xor(ga1, o); }
        u32x4 gq;
#pragma unroll
        for (int j = 0; j < 4; ++j) {
          const int src = 32 * (lane & 1) + 8 * j;
          const float v0 = __shfl(ga0, src), v1 = __shfl(ga1, src);
          gq[j] = __float_as_uint(__builtin_amdgcn_rcpf(1.f + __expf(-((lane & 2) ? v1 : v0))));
        }
        if (lane < 4) __builtin_amdgcn_raw_buffer_store_b128(gq, make_srd(p.ws + WS_GATE, S_ * 4), (T0 + 16 * G) * 4 + lane * 16, 0, 16);
        FENCE();
        __builtin_amdgcn_s_setprio(0);
      }
      STAMP(15); STAMP_RT(44);
      int cj, blk0;
      if (xi < 12) { cj = xi >> 2; blk0 = 6 * (xi & 3); } else { cj = 3 + ((xi - 12) >> 1); blk0 = 6 * (xi & 1); }
      loader2<6>(p, lds0, lane, lw, (xcd >> 1) * 32 + blk0, 5 * ch + cj, f_oth, fidx, gw_wave DIAG_PASS);
    }
    STAMP_OUT();
    return;
  }

  const int w = wid - 4;
  LGKM_BAR();
  if (is_attn == (PFMODE == 3)) {
    const char* pa; unsigned pstride;
    const int L = (xi * 4 + w) * 36 + (lane < 36 ? lane : 0);
    if (L < 528) { int r = (xcd >> 1) * 512 - 16 + L; r = r < 0 ? 0 : r; pa = (const char*)p.x + r * 1280; pstride = 128u; }
    else if (L < 1488) { const int t = L - 528, k = t / 15, j = t % 15; pa = (const char*)p.wqkv + k * 3840 + (j / 5) * 1280 + ch * 640 + (j % 5) * 128; pstride = 245760u; }
    else { const int t = (L < 2256 ? L : 1488) - 1488, k = t / 12, j = t % 12; pa = (const char*)p.w1 + k * 3072 + ch * 1536 + j * 128; pstride = 196608u; }
    pa += 3 * (size_t)pstride;
#pragma unroll
    for (int st = 3; st < 10; ++st) { touch(pa, lds0 + L_SCR); pa += pstride; }
  }
  __builtin_amdgcn_s_setprio(2);
  const int g = lane >> 4, q = (lane & 15) >> 2, pq = lane & 3, li = lane & 15;
  const unsigned lrow = 16 * (g >> 1) + 4 * (g & 1) + q;
  const srd_t sz = make_srd(p.ws, Z_BYTES);
  const unsigned vz = lane * 16u;
  if (is_attn) {
    unsigned wa[5];
#pragma unroll
    for (int i = 0; i < 4; ++i) wa[i] = lds0 + lrow * 480u + 40u * (i ^ w) + 8u * pq;
    wa[4] = lds0 + lrow * 480u + 40u * pq + 32u;
    unsigned xa[2], xh[2];
#pragma unroll
    for (int ks = 0; ks < 2; ++ks) {
      xh[ks] = lds0 + A_WB + li * 128u + 16u * ((4 * ks + g) ^ (lane & 7));
      xa[ks] = xh[ks] + 2048u * (1 + 2 * w);
    }
    f32x4 acc[15][2], hk0 = {}, hkx = {}, hv0 = {}, hvx = {};
#pragma unroll
    for (int u = 0; u < 15; ++u) { acc[u][0] = f32x4{}; acc[u][1] = f32x4{}; }
    bf16x8 wn[6], xn[3];
    unsigned wc_[5], xc_[2], hc_[2];
#define A_ADR(u, t) (wc_[(u) % 5] + (32 * ((u) / 15) + 8 * (t)) * 480u + (((u) % 15) / 5) * 160u)
#pragma unroll
    for (int i = 0; i < 5; ++i) { wc_[i] = wa[i]; OPAQUE(wc_[i]); }
#pragma unroll
    for (int i = 0; i < 2; ++i) { xc_[i] = xa[i]; hc_[i] = xh[i]; OPAQUE(xc_[i]); OPAQUE(hc_[i]); }
    prog_wait_sleep(f_oth, 1);
    STAMP(1); STAMP_RT(44);
    xn[0] = lds_b128(xc_[0]); xn[1] = lds_b128(xc_[0] + 2048u);
#pragma unroll
    for (int i = 0; i < 6; ++i) wn[i] = lds_tr8(A_ADR(i, 0), A_ADR(i, 1));
    xn[2] = lds_b128(hc_[0]);
    SB();
    unsigned sbn = A_STAGE;
    for (int st = 0; st < 10; ++st) {
      bf16x8 wf[30], xf[2][3];
      unsigned seen = 0;
#pragma unroll
      for (int i = 0; i < 6; ++i) wf[i] = wn[i];
#pragma unroll
      for (int i = 0; i < 3; ++i) xf[0][i] = xn[i];
#pragma unroll
      for (int u = 0; u < 30; ++u) {
        const int ks = u / 15, uu = u % 15, sec = uu / 5, tu = (u + 6) % 30;
        if (u == 24) {
          prog_wait(f_oth, seen, st < 9 ? st + 2 : 0);
          STAMPL(2 + st);
          SB();
        }
        if (sec < 2) acc[uu][0] = mfma(wf[u], xf[ks][0], acc[uu][0]); else acc[uu][0] = mfma(xf[ks][0], wf[u], acc[uu][0]);
        SB();
        const bf16x4 lo = lds_tr4(A_ADR(tu, 0));
        SB();
        if (sec < 2) acc[uu][1] = mfma(wf[u], xf[ks][1], acc[uu][1]); else acc[uu][1] = mfma(xf[ks][1], wf[u], acc[uu][1]);
        SB();
        const bf16x4 hi = lds_tr4(A_ADR(tu, 1));
        if (u < 24) wf[u + 6] = cat8(lo, hi); else wn[u - 24] = cat8(lo, hi);
        if (u >= 19 && u < 24) { wc_[u - 19] = wa[u - 19] + sbn; OPAQUE(wc_[u - 19]); }
        if (u == 12) { xc_[0] = xa[0] + sbn; OPAQUE(xc_[0]); }
        if (u == 13) { hc_[0] = xh[0] + sbn; OPAQUE(hc_[0]); }
        if (u == 16) { xc_[1] = xa[1] + sbn; OPAQUE(xc_[1]); }
        if (u == 17) { hc_[1] = xh[1] + sbn; OPAQUE(hc_[1]); }
        if (u == 23) prog_set(f_own, st + 1);
        SB();
        if (uu == 5) hk0 = mfma(wf[u], xf[ks][2], hk0);
        if (uu == 9) hkx = mfma(wf[u], xf[ks][2], hkx);
        if (uu == 10) hv0 = mfma(xf[ks][2], wf[u], hv0);
        if (uu == 14) hvx = mfma(xf[ks][2], wf[u], hvx);
        SB();
        if (u == 5) xf[1][0] = lds_b128(xc_[1]);
        if (u == 9) xf[1][1] = lds_b128(xc_[1] + 2048u);
        if (u == 10) xf[1][2] = lds_b128(hc_[1]);
        if (u == 20) seen = prog_read(f_oth);
        if (u == 24) xn[0] = lds_b128(xc_[0]);
        if (u == 25) xn[1] = lds_b128(xc_[0] + 2048u);
        if (u == 29) { xn[2] = lds_b128(hc_[0]); sbn = sbn == 2 * A_STAGE ? 0u : sbn + A_STAGE; }
        SB();
      }
    }
    STAMP(12); STAMP_RT(45);
    const unsigned f_cmp = f_oth + 1024u;
    const unsigned seen10 = prog_read(f_cmp);
    u32x4 Kt[4][2];
    u32x2 Vp[5][2];
#pragma unroll
    for (int bk = 1; bk >= 0; --bk) {
#pragma unroll
      for (int i = 0; i < 4; ++i) {
        const u32x2 km = pk4(acc[5 + i][bk]), kx = pk4(acc[9][bk]);
        Kt[i][bk] = u32x4{km[0], km[1], kx[0], kx[1]};
      }
#pragma unroll
      for (int i = 0; i < 5; ++i) Vp[i][bk] = pk4(acc[10 + i][bk]);
      if (bk == 1) {
        prog_wait(f_cmp, seen10, 10);
        const unsigned ek = lds0 + lane * 16u, ev = lds0 + 4096u + lane * 8u;
        if (w < 3) {
#pragma unroll
          for (int i = 0; i < 4; ++i) {
            *(LAS u32x4*)(size_t)(ek + (w + 1) * 8192u + (i ^ w) * 1024u) = Kt[i][1];
            *(LAS u32x2*)(size_t)(ev + (w + 1) * 8192u + (i ^ w) * 512u) = Vp[i][1];
          }
          *(LAS u32x2*)(size_t)(ev + (w + 1) * 8192u + 2048u) = Vp[4][1];
        }
        const u32x2 a = pk4(hk0), ax = pk4(hkx);
        *(LAS u32x4*)(size_t)(ek + w * 1024u) = u32x4{a[0], a[1], ax[0], ax[1]};
        *(LAS u32x2*)(size_t)(ev + w * 512u) = pk4(hv0);
        if (w == 0) *(LAS u32x2*)(size_t)(ev + 2048u) = pk4(hvx);
        prog_set(f_own, 11);
      }
    }
    const int par = li & 1;
    const bool cura = (4 * g + par) <= li, curb = (4 * g + par + 2) <= li;
    const unsigned hm = par ? 0xffff0000u : 0x0000ffffu;
    const unsigned ma0 = cura ? 0u : hm, ma1 = cura ? hm : 0u, mb0 = curb ? 0u : hm, mb1 = curb ? hm : 0u;
    const float SC = 0.22360679774997896f * 1.4426950408889634f, NEG = -1e9f * 1.4426950408889634f;
    const bool first = (rt == 0) && (w == 0);
    float pinf = __builtin_inff(); asm volatile("" : "+s"(pinf));
    unsigned mq[4], mv[4];
#pragma unroll
    for (int i = 0; i < 4; ++i) { mq[i] = (g == (i ^ w)) ? 0xffffffffu : 0u; mv[i] = (q == (i ^ w)) ? 0xffffffffu : 0u; }
    auto attend = [&](const int bk, const u32x4 (&Kprev)[4], const u32x2 (&Vprev)[5]) {
      const bool fm = first && bk == 0;
      const float sca = (fm && !cura) ? 0.f : SC, bia = (fm && !cura) ? NEG : 0.f;
      const float scb = (fm && !curb) ? 0.f : SC, bib = (fm && !curb) ? NEG : 0.f;
      const u32x2 Qx = pk4(acc[4][bk]);
      f32x4 s0[4], s1[4];
#pragma unroll
      for (int i = 0; i < 4; ++i) {
        const u32x2 Qm = pk4(acc[i][bk]);
        const u32x4 Qf = {Qm[0], Qm[1], Qx[0] & mq[i], Qx[1] & mq[i]};
        s0[i] = mfmau(Kprev[i], Qf, f32x4{}); s1[i] = mfmau(Kt[i][bk], Qf, f32x4{});
      }
      u32x4 Vx[4];
#pragma unroll
      for (int i = 0; i < 4; ++i) Vx[i] = u32x4{Vprev[4][0] & mv[i], Vprev[4][1] & mv[i], Vp[4][bk][0] & mv[i], Vp[4][bk][1] & mv[i]};
      float la[4], lb[4], mx[4];
#pragma unroll
      for (int i = 0; i < 4; ++i) {
        float e0 = s0[i][0], e2 = s0[i][2], f0 = s1[i][0], f2 = s1[i][2];
        asm("" : "+v"(e0), "+v"(e2), "+v"(f0), "+v"(f2));
        const float s0a = par ? s0[i][1] : e0, s0b = par ? s0[i][3] : e2;
        const float s1a = par ? s1[i][1] : f0, s1b = par ? s1[i][3] : f2;
        la[i] = (cura ? s1a : s0a) * sca + bia; lb[i] = (curb ? s1b : s0b) * scb + bib;
        mx[i] = vmax(la[i], lb[i]);
      }
      swap32(mx[0], mx[1]); swap32(mx[2], mx[3]);
      float m01 = vmax(mx[0], mx[1]), m23 = vmax(mx[2], mx[3]);
      swap16(m01, m23);
      float mA = vmax(m01, m23), mB = mA;
      swap16(mA, mB);
      mx[0] = mA; mx[1] = mA; mx[2] = mB; mx[3] = mB;
      swap32(mx[0], mx[1]); swap32(mx[2], mx[3]);
      float ea[4], eb[4], sm[4];
#pragma unroll
      for (int i = 0; i < 4; ++i) {
        ea[i] = __builtin_amdgcn_exp2f(la[i] - mx[i]); eb[i] = __builtin_amdgcn_exp2f(lb[i] - mx[i]);
        sm[i] = ea[i] + eb[i];
      }
      swap32(sm[0], sm[1]); swap32(sm[2], sm[3]);
      float t01 = sm[0] + sm[1], t23 = sm[2] + sm[3];
      swap16(t01, t23);
      float iA = __builtin_amdgcn_rcpf(t01 + t23), iB = iA;
      swap16(iA, iB);
      sm[0] = iA; sm[1] = iA; sm[2] = iB; sm[3] = iB;
      swap32(sm[0], sm[1]); swap32(sm[2], sm[3]);
      f32x4 om[4], ox = {};
#pragma unroll
      for (int i = 0; i < 4; ++i) {
        const float pa = ea[i] * sm[i], pb = eb[i] * sm[i];
        const unsigned ta = pk2(pa, pa), tb = pk2(pb, pb);
        const u32x4 P = {ta & ma0, tb & mb0, ta & ma1, tb & mb1};
        om[i] = mfmau(u32x4{Vprev[i][0], Vprev[i][1], Vp[i][bk][0], Vp[i][bk][1]}, P, f32x4{});
        ox = mfmau(Vx[i], P, ox);
      }
      const unsigned zrow = vz + (unsigned)((rt * 8 + 2 * w + bk) * KS2) * 1024u;
      const bool sw = w & 1;
#pragma unroll
      for (int i = 0; i < 4; i += 2) {
        const u32x2 o0 = pk4(om[i]), o1 = pk4(om[i + 1]);
        const u32x4 v = {sw ? o1[0] : o0[0], sw ? o1[1] : o0[1], sw ? o0[0] : o1[0], sw ? o0[1] : o1[1]};
        __builtin_amdgcn_raw_buffer_store_b128(v, sz, zrow + (unsigned)(2 * G + ((i ^ w) >> 1)) * 1024u, 0, 16);
      }
      __builtin_amdgcn_raw_buffer_store_b64(pk4(ox), sz, zrow + (unsigned)(16 + (G >> 1)) * 1024u + 8u * (G & 1), 0, 16);
    };
    {
      const u32x4 Kp0[4] = {Kt[0][0], Kt[1][0], Kt[2][0], Kt[3][0]};
      const u32x2 Vp0[5] = {Vp[0][0], Vp[1][0], Vp[2][0], Vp[3][0], Vp[4][0]};
      attend(1, Kp0, Vp0);
    }
    prog_wait(f_cmp, prog_read(f_cmp), 11);
    u32x4 Kh[4]; u32x2 Vh[5];
#pragma unroll
    for (int i = 0; i < 4; ++i) {
      Kh[i] = *(LAS u32x4*)(size_t)(lds0 + lane * 16u + w * 8192u + (i ^ w) * 1024u);
      Vh[i] = *(LAS u32x2*)(size_t)(lds0 + 4096u + lane * 8u + w * 8192u + (i ^ w) * 512u);
    }
    Vh[4] = *(LAS u32x2*)(size_t)(lds0 + 4096u + lane * 8u + w * 8192u + 2048u);
    prog_set(f_own, 12);
    attend(0, Kh, Vh);
    STAMP(13); STAMP_RT(46);
    int cj, blk0;
    if (xi < 6) { cj = xi >> 1; blk0 = 24 + 4 * (xi & 1); } else { const int t = xi - 6; cj = t < 5 ? 3 : 4; blk0 = 12 + 4 * (t < 5 ? t : t - 5); }
    compute2<4>(p, lds0, lane, w, (xcd >> 1) * 32 + blk0, 5 * ch + cj, f_own, fidx DIAG_PASS);
  } else {
    const int wr = w >> 1, wc = w & 1;
    const unsigned f = 2 * (g & 1) + (q >> 1);
    unsigned wa[6];
#pragma unroll
    for (int n = 0; n < 6; ++n) wa[n] = lds0 + lrow * 384u + 32u * ((6 * wc + n) ^ f) + 8u * pq;
    unsigned xa[2];
#pragma unroll
    for (int ks = 0; ks < 2; ++ks) xa[ks] = lds0 + M_WB + (64 * wr + li) * 128u + 16u * ((4 * ks + g) ^ (lane & 7));
    f32x4 acc[6][4];
    bf16x8 wn[3], xn[4];
    unsigned wc_[6], xc_[2];
#define M_ADR(u, t) (wc_[(u) % 6] + (32 * ((u) / 6) + 8 * (t)) * 384u)
#pragma unroll
    for (int i = 0; i < 6; ++i) { wc_[i] = wa[i]; OPAQUE(wc_[i]); }
#pragma unroll
    for (int i = 0; i < 2; ++i) { xc_[i] = xa[i]; OPAQUE(xc_[i]); }
    prog_wait_sleep(f_oth, 1);
    STAMP(1); STAMP_RT(44);
    wn[0] = lds_tr8(M_ADR(0, 0), M_ADR(0, 1));
#pragma unroll
    for (int i = 0; i < 4; ++i) xn[i] = lds_b128(xc_[0] + 2048u * i);
    wn[1] = lds_tr8(M_ADR(1, 0), M_ADR(1, 1)); wn[2] = lds_tr8(M_ADR(2, 0), M_ADR(2, 1));
#pragma unroll
    for (int n = 0; n < 6; ++n) {
      const f32x4 bi = bf4_to_f32(*(LAS u32x2*)(size_t)(lds0 + L_BIAS + (96 * wc + 16 * n + 4 * g) * 2));
#pragma unroll
      for (int m = 0; m < 4; ++m) acc[n][m] = bi;
    }
    SB();
    unsigned sbn = M_STAGE;
    for (int st = 0; st < 10; ++st) {
      bf16x8 wf[12], xf[2][4];
      unsigned seen = 0;
#pragma unroll
      for (int i = 0; i < 3; ++i) wf[i] = wn[i];
#pragma unroll
      for (int i = 0; i < 4; ++i) xf[0][i] = xn[i];
#pragma unroll
      for (int u = 0; u < 12; ++u) {
        const int ks = u / 6, n = u % 6, tu = (u + 3) % 12;
        if (u == 8) {
          prog_wait(f_oth, seen, st < 9 ? st + 2 : 0);
          STAMPL(2 + st);
          SB();
        }
        acc[n][0] = mfma(wf[u], xf[ks][0], acc[n][0]);
        SB();
        const bf16x4 lo = lds_tr4(M_ADR(tu, 0));
        SB();
        acc[n][1] = mfma(wf[u], xf[ks][1], acc[n][1]);
        SB();
        const bf16x4 hi = lds_tr4(M_ADR(tu, 1));
        if (u < 9) wf[u + 3] = cat8(lo, hi); else wn[u - 9] = cat8(lo, hi);
        SB();
        acc[n][2] = mfma(wf[u], xf[ks][2], acc[n][2]);
        SB();
        if (u < 4) xf[1][u] = lds_b128(xc_[1] + 2048u * u);
        if (u == 5) seen = prog_read(f_oth);
        if (u >= 8) xn[u - 8] = lds_b128(xc_[0] + 2048u * (u - 8));
        SB();
        acc[n][3] = mfma(wf[u], xf[ks][3], acc[n][3]);
        SB();
        if (u >= 3 && u < 9) { wc_[u - 3] = wa[u - 3] + sbn; OPAQUE(wc_[u - 3]); }
        if (u == 1) { xc_[0] = xa[0] + sbn; OPAQUE(xc_[0]); }
        if (u == 9) { xc_[1] = xa[1] + sbn; OPAQUE(xc_[1]); }
        if (u == 8) prog_set(f_own, st + 1);
        if (u == 11) sbn = sbn == 2 * M_STAGE ? 0u : sbn + M_STAGE;
        SB();
      }
    }
    STAMP(12); STAMP_RT(45);
    prog_set(f_own, 12);
    const float C1 = -2.f * 0.7978845608028654f * 1.4426950408889634f, C2 = C1 * 0.044715f;
#pragma unroll
    for (int m = 0; m < 4; ++m) {
      const unsigned zrow = vz + (unsigned)((rt * 8 + 4 * wr + m) * KS2 + 20 + 6 * G + 3 * wc) * 1024u;
#pragma unroll
      for (int j = 0; j < 3; ++j) {
        u32x4 o;
#pragma unroll
        for (int e = 0; e < 2; ++e) {
#pragma unroll
          for (int k = 0; k < 2; ++k) {
            const f32x2 x = {acc[2 * j + e][m][2 * k], acc[2 * j + e][m][2 * k + 1]};
            const f32x2 t = x * (x * x * C2 + C1);
            f32x2 d = {__builtin_amdgcn_exp2f(t[0]), __builtin_amdgcn_exp2f(t[1])};
            d = d + 1.f;
            f32x2 r = {__builtin_amdgcn_rcpf(d[0]), __builtin_amdgcn_rcpf(d[1])};
            r = r * x;
            o[2 * e + k] = pk2(r[0], r[1]);
          }
        }
        __builtin_amdgcn_raw_buffer_store_b128(o, sz, zrow + 1024u * j, 0, 16);
      }
    }
    STAMP(13); STAMP_RT(46);
    int cj, blk0;
    if (xi < 12) { cj = xi >> 2; blk0 = 6 * (xi & 3); } else { cj = 3 + ((xi - 12) >> 1); blk0 = 6 * (xi & 1); }
    compute2<6>(p, lds0, lane, w, (xcd >> 1) * 32 + blk0, 5 * ch + cj, f_own, fidx DIAG_PASS);
  }
  STAMP(59);
  STAMP_OUT();
}

extern "C" void kernel_launch(void* const* d_in, const int* in_sizes, int n_in, void* d_out, int out_size, void* d_ws,
                              size_t ws_size, hipStream_t stream) {
  (void)in_sizes; (void)n_in; (void)out_size; (void)ws_size;
  (void)hipFuncSetAttribute((const void*)fused, hipFuncAttributeMaxDynamicSharedMemorySize, L_TOTAL);
  Args a{d_in[0], d_in[1], d_in[4], d_in[5], d_in[8], d_in[2], d_in[6], d_in[7], d_out, (char*)d_ws};
  fused<<<256, 512, L_TOTAL, stream>>>(a);
}
```

```cpp
#include <hip/hip_runtime.h>
#include <hip/hip_bf16.h>
#include <stdint.h>
#include <type_traits>

typedef __attribute__((ext_vector_type(8))) __bf16 bf16x8;
typedef __attribute__((ext_vector_type(4))) __bf16 bf16x4;
typedef __attribute__((ext_vector_type(2))) __bf16 bf16x2;
typedef __attribute__((ext_vector_type(4))) float f32x4;
typedef __attribute__((ext_vector_type(2))) float f32x2;
typedef __attribute__((ext_vector_type(4))) unsigned u32x4;
typedef __attribute__((ext_vector_type(2))) unsigned u32x2;
typedef __amdgpu_buffer_rsrc_t srd_t;
typedef __attribute__((address_space(1))) unsigned long long gu64;
typedef __attribute__((address_space(1))) unsigned gu32;
#define LAS __attribute__((address_space(3)))
#define DI __device__ __forceinline__
#define RLX_AGENT __ATOMIC_RELAXED, __HIP_MEMORY_SCOPE_AGENT
#define RLX_WG __ATOMIC_RELAXED, __HIP_MEMORY_SCOPE_WORKGROUP
#define SB() __builtin_amdgcn_sched_barrier(0)
#define OPAQUE(x) asm volatile("" : "+v"(x))
#define FENCE() asm volatile("" ::: "memory")
#define WAITV0() asm volatile("s_waitcnt vmcnt(0)" ::: "memory")
#define LGKM_BAR() asm volatile("s_waitcnt lgkmcnt(0)\n\ts_barrier" ::: "memory")

constexpr int S_ = 2048, D_ = 640, F_ = 1536;
constexpr int KS2 = 68;
constexpr unsigned Z_BYTES = 128u * KS2 * 1024u;
constexpr unsigned WS_GATE = Z_BYTES, WS_FLAGS = WS_GATE + 8192, WS_EPOCH = WS_FLAGS + 2048;
constexpr unsigned SPIN_CAP = 1u << 18;
constexpr unsigned A_WB = 30720, A_STAGE = 49152;
constexpr unsigned M_WB = 24576, M_STAGE = 40960;
constexpr unsigned L_MISC = 3 * A_STAGE;
constexpr unsigned L_WROW = L_MISC + 2048;
constexpr unsigned L_SCR = L_WROW + 1024, L_GATE = L_SCR + 256, L_B2 = L_GATE + 1024, L_BIAS = L_B2 + 1024, L_GW = L_BIAS + 1024;
constexpr unsigned L_WORDS = L_GW + 2048, L_TOTAL = L_WORDS + 64;

struct Args { const void* x; const void* wqkv; const void* w1; const void* b1; const void* gw; const void* wo; const void* w2; const void* b2; void* out; char* ws; };

DI srd_t make_srd(const void* p, unsigned bytes) { return __builtin_amdgcn_make_buffer_rsrc((void*)p, (short)0, (int)bytes, 0x00020000); }
DI void dma16(unsigned voff, srd_t srd, unsigned soff, unsigned lds) {
  asm volatile("s_mov_b32 m0, %0\n\ts_nop 0\n\tbuffer_load_dwordx4 %1, %2, %3 offen lds" :: "s"(lds), "v"(voff), "s"(srd), "s"(soff) : "memory");
}
template <unsigned HI> DI void dma16m(unsigned voff, srd_t srd, unsigned soff, unsigned lds) {
  asm volatile("s_mov_b32 m0, %0\n\ts_mov_b32 exec_hi, %4\n\tbuffer_load_dwordx4 %1, %2, %3 offen lds\n\ts_mov_b32 exec_hi, -1"
               :: "s"(lds), "v"(voff), "s"(srd), "s"(soff), "i"(HI) : "memory");
}
DI void dma16c1(unsigned voff, srd_t srd, unsigned soff, unsigned lds) {
  asm volatile("s_mov_b32 m0, %0\n\ts_mov_b64 exec, 1\n\tbuffer_load_dwordx4 %1, %2, %3 offen sc1 lds\n\ts_mov_b64 exec, -1"
               :: "s"(lds), "v"(voff), "s"(srd), "s"(soff) : "memory");
}
__device__ const unsigned g_seq[16] = {0, 1, 2, 3, 4, 5, 6, 7, 8, 9, 10, 11, 12, 13, 14, 15};
DI void dma4(unsigned voff, srd_t srd, unsigned soff, unsigned lds) {
  asm volatile("s_mov_b32 m0, %0\n\ts_mov_b64 exec, 1\n\tbuffer_load_dword %1, %2, %3 offen lds\n\ts_mov_b64 exec, -1"
               :: "s"(lds), "v"(voff), "s"(srd), "s"(soff) : "memory");
}
DI void touch(const char* addr, unsigned lds_scratch) {
  asm volatile("s_mov_b32 m0, %0\n\ts_nop 0\n\tglobal_load_lds_dword %1, off" :: "s"(lds_scratch), "v"(addr) : "memory");
}
DI bf16x4 lds_tr4(unsigned a) { return __builtin_amdgcn_ds_read_tr16_b64_v4bf16((LAS bf16x4*)(size_t)a); }
DI bf16x8 cat8(bf16x4 lo, bf16x4 hi) { return __builtin_shufflevector(lo, hi, 0, 1, 2, 3, 4, 5, 6, 7); }
DI bf16x8 lds_tr8(unsigned a0, unsigned a1) { return cat8(lds_tr4(a0), lds_tr4(a1)); }
DI bf16x8 lds_b128(unsigned a) { return *(LAS bf16x8*)(size_t)a; }
DI f32x4 mfma(bf16x8 a, bf16x8 b, f32x4 c) { return __builtin_amdgcn_mfma_f32_16x16x32_bf16(a, b, c, 0, 0, 0); }
DI f32x4 mfmau(u32x4 a, u32x4 b, f32x4 c) { return mfma(__builtin_bit_cast(bf16x8, a), __builtin_bit_cast(bf16x8, b), c); }
#define MF(c, a, b) asm volatile("v_mfma_f32_16x16x32_bf16 %0, %1, %2, %0" : "+v"(c) : "v"(a), "v"(b))
#define MF0(c, a, b) asm volatile("v_mfma_f32_16x16x32_bf16 %0, %1, %2, 0" : "=&v"(c) : "v"(a), "v"(b))
DI unsigned pk2(float a, float b) { bf16x2 v = {(__bf16)a, (__bf16)b}; return __builtin_bit_cast(unsigned, v); }
DI u32x2 pk4(f32x4 v) { return u32x2{pk2(v[0], v[1]), pk2(v[2], v[3])}; }
#define vmax(a, b) __builtin_amdgcn_fmed3f(a, b, pinf)
DI f32x4 bf4_to_f32(u32x2 v) {
  return f32x4{__uint_as_float(v[0] << 16), __uint_as_float(v[0] & 0xffff0000u), __uint_as_float(v[1] << 16), __uint_as_float(v[1] & 0xffff0000u)};
}
DI void swap32(float& a, float& b) {
  auto r = __builtin_amdgcn_permlane32_swap(__float_as_uint(a), __float_as_uint(b), false, false);
  a = __uint_as_float(r[0]); b = __uint_as_float(r[1]);
}
DI void swap16(float& a, float& b) {
  auto r = __builtin_amdgcn_permlane16_swap(__float_as_uint(a), __float_as_uint(b), false, false);
  a = __uint_as_float(r[0]); b = __uint_as_float(r[1]);
}
template <int N, class F> DI void static_for(F&& f) {
  if constexpr (N > 0) { static_for<N - 1>(f); f(std::integral_constant<int, N - 1>{}); }
}

DI void prog_set(unsigned row_addr, unsigned v) { FENCE(); __hip_atomic_store((LAS unsigned*)(size_t)row_addr, v, RLX_WG); FENCE(); }
DI unsigned prog_read(unsigned addr) { return __hip_atomic_load((LAS unsigned*)(size_t)addr, RLX_WG); }
DI void prog_wait(unsigned addr, unsigned seen, unsigned need) {
  if (__builtin_expect(__builtin_amdgcn_ballot_w64(seen < need) != 0, 0)) {
    unsigned long long m;
    do {
      unsigned v;
      asm volatile("ds_read_b32 %0, %1\n\ts_waitcnt lgkmcnt(0)" : "=v"(v) : "v"(addr) : "memory");
      m = __builtin_amdgcn_ballot_w64(v < need);
    } while (m != 0);
  }
  FENCE();
}
DI void prog_wait_sleep(unsigned addr, unsigned need) {
  while (__builtin_amdgcn_ballot_w64(prog_read(addr) < need) != 0) __builtin_amdgcn_s_sleep(1);
  FENCE();
}
template <unsigned NEED> DI void prog_wait_inl(unsigned addr, unsigned seen) {
  asm volatile("v_cmp_gt_u32 vcc, %2, %0\n\ts_cbranch_vccz 2%=f\n1%=:\n\ts_sleep 1\n\tds_read_b32 %0, %1\n\ts_waitcnt lgkmcnt(0)\n\t"
               "v_cmp_gt_u32 vcc, %2, %0\n\ts_cbranch_vccnz 1%=b\n2%=:"
               : "+v"(seen) : "v"(addr), "i"(NEED) : "vcc", "memory");
}
DI unsigned swz23(unsigned k) { return (k & ~12u) | ((k & 4u) << 1) | ((k & 8u) >> 1); }

DI unsigned long long get_epoch(const unsigned lds0) { return *(LAS unsigned long long*)(size_t)(lds0 + L_WORDS + 16); }
DI unsigned long long get_tag(const unsigned lds0) { return (get_epoch(lds0) + 1) * 0x9E3779B97F4A7C15ull; }
template <int YOUNGER = 0>
DI void publish(const Args& p, const unsigned lds0, const int lane, const int fidx, const unsigned last) {
  asm volatile("s_waitcnt vmcnt(%0)" :: "i"(YOUNGER) : "memory");
  if (lane == 0) {
    const unsigned old = __hip_atomic_fetch_add((LAS unsigned*)(size_t)(lds0 + L_WORDS), 1u, RLX_WG);
    if (old == last) __hip_atomic_store((gu64*)(p.ws + WS_FLAGS) + fidx, get_tag(lds0), RLX_AGENT);
  }
  FENCE();
}
DI void prefetch_w2(const Args& p, const unsigned lds0, const int lane, const int wv128, const int ch) {
#pragma unroll
  for (int j = 0; j < 2; ++j) {
    int L = (j * 128 + wv128) * 43 + lane;
    L = (L < 10880 && lane < 43) ? L : 0;
    const int r = L / 5, cl = L % 5;
    touch((r < 1536 ? (const char*)p.w2 + r * 1280 : (const char*)p.wo + (r - 1536) * 1280) + ch * 640 + cl * 128, lds0 + L_SCR);
  }
}

constexpr int NW2 = 18;
template <int NB> constexpr int zdepth() { return NB == 8 ? 6 : 12; }

DI int k_slice(const int G0, const int j) { return ((G0 ^ ((j & 1) << 2)) & 4) | ((G0 + (j >> 1)) & 3); }
DI int k_group(const int G0, const int u) { return (2 * G0 + u) & 7; }
template <int NB>
DI void loader2(const Args& p, const unsigned lds0, const int lane, const int lw, const int rt, const int cn, const int G0, const unsigned f_cmp,
                const int fidx, const bool gw_wave) {
  const unsigned f_oth = (lane & 4) ? lds0 + L_WORDS + 8 : f_cmp;
  const srd_t so = make_srd(p.wo, D_ * D_ * 2);
  const srd_t s2 = make_srd(p.w2, F_ * D_ * 2);
  const unsigned rho = 8 * lw + (lane >> 3), t = rho >> 4, gg = (rho >> 2) & 3, qq = rho & 3;
  const unsigned cpos = lane & 7, sch = (((cpos >> 1) ^ ((rho >> 1) & 3)) << 1) | (cpos & 1);
  const unsigned cb = 128u * cn + 16u * sch;
  const unsigned vA = (20 * t + (rho & 15)) * 1280u + cb;
  const unsigned vB = (80 * t + 20 * gg + 16 + qq) * 1280u + cb;
  const unsigned vC = rho * 1280u + cb;
  const unsigned vz = lane * 16u;
  const unsigned wl = lds0 + 1024u * lw;
  const unsigned f_w = lds0 + L_WROW + lw * 256u + lane * 4u;
  auto w2off = [&](const int k) { return (unsigned)(6 * k_slice(G0, k / 6) + k % 6) * 40960u; };
  u32x4 wreg[2 * (34 - NW2)];
  auto load_wreg = [&]() {
#pragma unroll
    for (int k = 2 * NW2; k < 68; ++k) {
      wreg[k - 2 * NW2] = k < 48 ? __builtin_amdgcn_raw_buffer_load_b128(s2, vC, w2off(k), 0)
                        : (k < 64 ? __builtin_amdgcn_raw_buffer_load_b128(so, vA, 51200 * (2 * k_group(G0, (k - 48) >> 1) + (k & 1)), 0)
                                  : __builtin_amdgcn_raw_buffer_load_b128(so, vB, 204800 * (k - 64), 0));
    }
  };
  prog_wait_sleep(f_oth, 9);
#pragma unroll
  for (int k = 0; k < 24; ++k) dma16(vC, s2, w2off(k), wl + 49152u + 4096u * k);
  if (gw_wave) publish<24>(p, lds0, lane, fidx, 4);
  WAITV0();
  prog_set(f_w, 10 + 12);
  const unsigned long long tag = get_tag(lds0);
  const gu64* fl = (const gu64*)(p.ws + WS_FLAGS) + rt * 16 + ((lane & 8) ? 8 + k_group(G0, lane & 7) : k_slice(G0, lane & 7));
  int last = 0;
  auto poll = [&]() {
    const unsigned long long v = __hip_atomic_load(fl, RLX_AGENT);
    const int n = __builtin_ctz(~((unsigned)__builtin_amdgcn_ballot_w64(v == tag) & 0xffffu));
    const int pub = n == 16 ? 63 : (n <= 8 ? 3 * n : 16 + n) + 12 - zdepth<NB>();
    if (pub > last) {
      last = pub;
      FENCE();
      __hip_atomic_fetch_max((LAS unsigned*)(size_t)(lds0 + L_WORDS + 4), (unsigned)pub, RLX_WG);
      FENCE();
    }
  };
  while (__builtin_amdgcn_readfirstlane(prog_read(lds0 + L_WORDS)) < (NB == 2 ? 4u : 5u)) {
    if (NB == 2 && lw == 0 && last < 36 - zdepth<NB>()) poll(); else __builtin_amdgcn_s_sleep(1);
  }
  FENCE();
  if (NB == 2 && lw == 0) {
#pragma nounroll
    for (unsigned spins = 0; spins < SPIN_CAP && last < 12; ++spins) poll();
  }
#pragma unroll
  for (int k = 24; k < 36; ++k) dma16(vC, s2, w2off(k), wl + 4096u * (k - 24));
  load_wreg();
  if (lw == 1) dma16(vz, make_srd(p.b2, D_ * 2), cn * 128u, lds0 + L_B2);
  if (lw == 0) {
    poll();
    prog_set(f_w, 10 + NW2);
#pragma nounroll
    for (unsigned spins = 0; spins < SPIN_CAP && last < 63; ++spins) { __builtin_amdgcn_s_sleep(1); poll(); }
    FENCE();
    __hip_atomic_store((LAS unsigned*)(size_t)(lds0 + L_WORDS + 4), 63u, RLX_WG);
  }
  __builtin_amdgcn_s_waitcnt(0x0F70);
  prog_set(f_w, 10 + NW2);
  const unsigned wv = wl + vz;
  unsigned seen = prog_read(f_cmp);
  static_for<34 - NW2>([&](auto jc) {
    constexpr int j = decltype(jc)::value;
    prog_wait_inl<13 + j>(f_cmp, seen);
    seen = prog_read(f_cmp);
    *(LAS u32x4*)(size_t)(wv + ((j + 6) % NW2) * 8192u) = wreg[2 * j];
    *(LAS u32x4*)(size_t)(wv + ((j + 6) % NW2) * 8192u + 4096u) = wreg[2 * j + 1];
    prog_set(f_w, 11 + NW2 + j);
  });
  if (blockIdx.x == 0 && lw == 0) {
    const gu64* fa = (const gu64*)(p.ws + WS_FLAGS) + lane;
#pragma nounroll
    for (unsigned spins = 0; spins < SPIN_CAP; ++spins) {
      bool ok = true;
#pragma unroll
      for (int j = 0; j < 4; ++j) ok &= __hip_atomic_load(fa + 64 * j, RLX_AGENT) == tag;
      if (__builtin_amdgcn_ballot_w64(!ok) == 0) break;
      __builtin_amdgcn_s_sleep(2);
    }
    if (lane == 0) __hip_atomic_store((gu64*)(p.ws + WS_EPOCH), get_epoch(lds0) + 1, RLX_AGENT);
  }
}

template <int NB>
DI void compute2(const Args& p, const unsigned lds0, const int lane, const int w, const int tb0, const int cn, const int G0, const unsigned f_own,
                 const int fidx) {
  constexpr int MB = NB / 2, ZD = zdepth<NB>();
  constexpr bool OWN = NB == 8;
  const unsigned f_w = lds0 + L_WROW + (lane & 3) * 260u;
  const unsigned f_oth = (lane & 4) ? lds0 + L_WORDS + 4 : f_w;
  const unsigned f_cmp = lds0 + L_MISC + 1024u + (lane & 3) * 260u;
  const int wr = w >> 1, e = w & 1;
  const int g = lane >> 4, q = (lane & 15) >> 2, pq = lane & 3, li = lane & 15;
  const srd_t sz = make_srd(p.ws, Z_BYTES);
  unsigned zv[MB];
#pragma unroll
  for (int m = 0; m < MB; ++m) zv[m] = (unsigned)(((tb0 + MB * wr + m) * KS2 + e) * 1024) + lane * 16u;
  unsigned wa[3][4];
  {
    const unsigned r = 4 * g + q;
#pragma unroll
    for (int n = 0; n < 4; ++n) {
      wa[0][n] = lds0 + e * 4096u + r * 128u + 32u * (n ^ ((r >> 1) & 3)) + 8u * pq;
      wa[1][n] = wa[0][n] + 49152u; wa[2][n] = wa[0][n] + 98304u;
      OPAQUE(wa[0][n]); OPAQUE(wa[1][n]); OPAQUE(wa[2][n]);
    }
  }
  f32x4 acc[4][MB];
  u32x2 bs[4][MB];
  u32x4 zf[ZD][MB];
  bf16x8 wf[2][4];
  auto zoff = [&](const int s) { return (s < 24 ? 20 + 6 * k_slice(G0, s / 3) + 2 * (s % 3) : (s < 32 ? 2 * k_group(G0, s - 24) : 2 * s - 48)) * 1024; };
#define C2_ZLD(s, m) __builtin_amdgcn_raw_buffer_load_b128(sz, zv[m], zoff(s), 16)
#define C2_WAD(s, n) (wa[(((s) + 6) % NW2) / 6][n] + ((((s) + 6) % NW2) % 6) * 8192u)
  const bool early = !OWN && __builtin_amdgcn_ballot_w64(prog_read(f_oth) < 12u) == 0;
  FENCE();
  if (!early) {
    publish(p, lds0, lane, fidx, OWN ? 4 : 3);
    if (OWN) {
      while (__builtin_amdgcn_readfirstlane(prog_read(lds0 + L_WORDS)) < 5u) __builtin_amdgcn_s_sleep(1);
      prog_wait_sleep(f_w, 12);
    } else prog_wait_sleep(f_oth, 12);
  }
#pragma unroll
  for (int s = 0; s < (OWN ? 3 : ZD); ++s) {
#pragma unroll
    for (int m = 0; m < MB; ++m) zf[s][m] = C2_ZLD(s, m);
    SB();
  }
  if (early) { publish<ZD * MB>(p, lds0, lane, fidx, 3); }
  unsigned seen = OWN ? 63u : prog_read(f_oth);
  SB();
#pragma unroll
  for (int n = 0; n < 4; ++n) wf[0][n] = lds_tr8(C2_WAD(0, n), C2_WAD(0, n) + 2048u);
  prog_set(f_own, 13);
  unsigned one = 1; OPAQUE(one);
  const int r0 = w * (NB * 4) + (lane >> 3);
  float gt[MB], g2[MB];
  static_for<34>([&](auto sc) {
    constexpr int st = decltype(sc)::value, pp = st & 1, sn = st < 33 ? st + 1 : 33;
    if constexpr (OWN && st == 3) {
      if (w == 0) {
        const unsigned long long tag = get_tag(lds0);
        const gu64* fl = (const gu64*)(p.ws + WS_FLAGS) + (tb0 >> 3) * 16 + ((lane & 8) ? 8 + k_group(G0, lane & 7) : k_slice(G0, lane & 7));
        int n = 0;
#pragma nounroll
        for (unsigned spins = 0; spins < SPIN_CAP && n < 3; ++spins)
          n = __builtin_ctz(~((unsigned)__builtin_amdgcn_ballot_w64(__hip_atomic_load(fl, RLX_AGENT) == tag) & 0xffffu));
        FENCE();
        __hip_atomic_fetch_max((LAS unsigned*)(size_t)(lds0 + L_WORDS + 4), (unsigned)(n == 16 ? 63 : (n < 3 ? 15 : (n <= 8 ? 3 * n : 16 + n) + 12 - ZD)), RLX_WG);
        FENCE();
      }
    }
    if constexpr (st < 33 && !(OWN && st < 3)) prog_wait_inl<st + 12>(f_oth, seen);
    if constexpr (OWN && st == 3) {
#pragma unroll
      for (int s = 3; s < 3 + ZD; ++s) {
#pragma unroll
        for (int m = 0; m < MB; ++m) zf[s % ZD][m] = C2_ZLD(s, m);
        SB();
      }
    }

    SB();
    bf16x4 lo[4], hi[4];
    u32x4 zc[MB];
#pragma unroll
    for (int m = 0; m < MB; ++m) zc[m] = zf[st % ZD][m];
#define C2_LO(n) lo[n] = lds_tr4(C2_WAD(sn, n))
#define C2_HI(n) do { hi[n] = lds_tr4(C2_WAD(sn, n) + 2048u); wf[pp ^ 1][n] = cat8(lo[n], hi[n]); } while (0)
#define C2_DONE() do { FENCE(); __hip_atomic_fetch_add((LAS unsigned*)(size_t)f_own, one, RLX_WG); FENCE(); } while (0)
    if (st == 24 && MB == 1) { asm volatile("s_nop 7\n\ts_nop 7" ::: "memory"); SB(); }
#pragma unroll
    for (int k = 0; k < 4 * MB; ++k) {
      const int n = k / MB, m = k % MB;
      if (st == 24) { bs[n][m] = pk4(acc[n][m]); OPAQUE(bs[n][m]); SB(); }
      if (st == 0 || st == 24) MF0(acc[n][m], wf[pp][n], zc[m]); else MF(acc[n][m], wf[pp][n], zc[m]);
      SB();
      if (st < 33) {
        if (MB == 1) {
          if (k == 0) { C2_LO(0); C2_HI(0); seen = prog_read(f_oth); }
          if (k == 1) { C2_LO(1); C2_HI(1); }
          if (k == 2) { C2_LO(2); C2_HI(2); }
          if (k == 3) { C2_LO(3); C2_HI(3); C2_DONE(); }
        } else {
          if (k == 0) { C2_LO(0); seen = prog_read(f_oth); }
          if (k == 1) C2_HI(0);
          if (k == 3) C2_LO(1);
          if (k == 4) C2_HI(1);
          if (k == 5) C2_LO(2);
          if (k == 7) C2_HI(2);
          if (k == 8) C2_LO(3);
          if (k == 9) { C2_HI(3); C2_DONE(); }
        }
      }
      if (st == 30 && k == 1) {
        const srd_t sg = make_srd(p.ws + WS_GATE, S_ * 4);
#pragma unroll
        for (int m2 = 0; m2 < MB; ++m2) {
          gt[m2] = __uint_as_float(__builtin_amdgcn_raw_buffer_load_b32(sg, ((MB * wr + m2) * 16 + li) * 4, tb0 * 64, 16));
          g2[m2] = __uint_as_float(__builtin_amdgcn_raw_buffer_load_b32(sg, (r0 + 8 * m2) * 4, tb0 * 64, 16));
        }
      }
      if (k % 4 == 2 && st > (OWN ? 3 : 0) && st + ZD - 1 < 34) zf[(st - 1) % ZD][k / 4] = C2_ZLD(st + ZD - 1, k / 4);
      SB();
    }
  });
  const unsigned seen2 = prog_read(f_cmp);
  asm volatile("s_nop 7\n\ts_nop 7" ::: "memory");
  SB();
  prog_wait(f_cmp, seen2, 13 + 27);
  constexpr unsigned HALF = NB * 16 * 272;
  const unsigned xw = lds0 + 49152u + e * HALF + (wr * MB * 16 + li) * 272u + g * 16u;
#pragma unroll
  for (int m = 0; m < MB; ++m)
#pragma unroll
    for (int n = 0; n < 4; ++n) *(LAS f32x4*)(size_t)(xw + m * 16 * 272u + n * 64u) = acc[n][m] * gt[m] + bf4_to_f32(bs[n][m]) * (1.f - gt[m]);
  prog_set(f_own, 50);
  const u32x4 bv = *(LAS u32x4*)(size_t)(lds0 + L_B2 + (lane & 7) * 16u);
  prog_wait(f_cmp, prog_read(f_cmp), 50);
  const unsigned xr = lds0 + 49152u + r0 * 272u + (lane & 7) * 32u;
  char* ob = (char*)p.out + (size_t)(tb0 * 16 * D_ + 64 * cn) * 2;
  const unsigned vo = (r0 * D_ + (lane & 7) * 8) * 2;
  const f32x4 b0 = bf4_to_f32(u32x2{bv[0], bv[1]}), b1 = bf4_to_f32(u32x2{bv[2], bv[3]});
#pragma unroll
  for (int j = 0; j < NB / 2; ++j) {
    const unsigned a = xr + j * 8 * 272u;
    const f32x4 lo = *(LAS f32x4*)(size_t)a + *(LAS f32x4*)(size_t)(a + HALF) + b0 * (1.f - g2[j]);
    const f32x4 hi = *(LAS f32x4*)(size_t)(a + 16u) + *(LAS f32x4*)(size_t)(a + HALF + 16u) + b1 * (1.f - g2[j]);
    const u32x2 pl = pk4(lo), ph = pk4(hi);
    *(u32x4*)(ob + (size_t)(j * 8 * D_ * 2) + (size_t)vo) = u32x4{pl[0], pl[1], ph[0], ph[1]};
  }
}

__global__ __launch_bounds__(512, 2) void fused(Args p) {
  extern __shared__ __attribute__((aligned(16))) char shm[];
  const unsigned lds0 = (unsigned)(uintptr_t)(LAS char*)shm;
  const int lane = threadIdx.x & 63;
  const int wid = __builtin_amdgcn_readfirstlane(threadIdx.x >> 6);
  const int b = blockIdx.x, xcd = b & 7, idx = b >> 3;
  const int rt = (xcd >> 1) * 4 + (idx & 3), c = idx >> 2, ch = xcd & 1;
  const bool is_attn = c < 4;
  const int G = 4 * ch + (c & 3);
  const int T0 = rt * 128;
  const int xi = idx & 15;
  const int fidx = rt * 16 + (is_attn ? 8 + G : G);
  const unsigned f_own = lds0 + L_MISC + wid * 256u + lane * 4u;
  const unsigned f_oth = lds0 + L_MISC + (wid < 4 ? 1024u : 0u) + (lane & 3) * 260u;
  prog_set(f_own, 0u);

  if (__builtin_expect(wid < 4, 1)) {
    const int lw = wid;
    const srd_t sx = make_srd(p.x, S_ * D_ * 2);
    const srd_t sq = make_srd(g_seq, 64);
    const unsigned vzero = 0, f_row = lds0 + L_MISC + wid * 260u;
    const unsigned vx = (lane >> 3) * 1280u + 16u * ((lane & 7) ^ (lane >> 3));
    prog_set(lds0 + L_WROW + lw * 256u + lane * 4u, 0u);
    if (lw == 0 && lane == 0) *(LAS u32x4*)(size_t)(lds0 + L_WORDS) = u32x4{0u, 0u, is_attn ? 63u : 0u, 0u};
    if (is_attn) {
      const srd_t sw = make_srd(p.wqkv, D_ * 3 * D_ * 2);
      const unsigned r2 = lane >= 30 ? 1u : 0u, rem = lane - 30u * r2, sec = (rem >= 20 ? 2u : (rem >= 10 ? 1u : 0u));
      const unsigned vw = r2 * 3840u + sec * 1280u + 16u * (rem - 10u * sec);
      const int hr = 4 * lw + (lane >> 3);
      const int ht = T0 - 16 + hr;
      const unsigned vh = (unsigned)(ht < 0 ? 0 : ht) * 1280u + 16u * ((lane & 7) ^ (hr & 7));
      auto issue = [&](int st, unsigned lb) {
        const unsigned sow = st * 245760u + 160u * G, sox = st * 128u + (unsigned)T0 * 1280u;
        dma16m<0u>(vh, sx, st * 128u, lb + A_WB + 512u * lw);
#pragma unroll
        for (int i = 0; i < 4; ++i) { const unsigned n = 4 * lw + i; dma16(vx, sx, sox + n * 10240u, lb + A_WB + 2048u + 1024u * n); }
#pragma unroll
        for (int i = 0; i < 8; ++i) { const unsigned n = 8 * lw + i; dma16m<0x0fffffffu>(vw, sw, sow + swz23(2 * n) * 3840u, lb + 960u * n); }
      };
      issue(0, lds0);
      if (lw == 0) dma16c1(vzero, make_srd(p.ws + WS_EPOCH, 16), 0u, lds0 + L_WORDS + 16);
      asm volatile("s_waitcnt lgkmcnt(0)" ::: "memory");
      dma4(vzero, sq, 4, f_row);
      LGKM_BAR();
      unsigned lb = lds0 + A_STAGE;
      for (int st = 1; st < 10; ++st) {
        if (st >= 3) prog_wait_sleep(f_oth, st - 2);
        issue(st, lb);
        dma4(vzero, sq, 4 * st + 4, f_row);
        if (st == 4) { prefetch_w2(p, lds0, lane, idx * 4 + lw, ch); prefetch_w2(p, lds0, lane, (idx + 16) * 4 + lw, ch); }
        lb = lb == lds0 + 2 * A_STAGE ? lds0 : lb + A_STAGE;
      }
      loader2<2>(p, lds0, lane, lw, rt, 5 * ch + 4, 0, f_oth, fidx, false);
    } else {
      const srd_t sw = make_srd(p.w1, D_ * F_ * 2);
      unsigned vw[3];
#pragma unroll
      for (int i = 0; i < 3; ++i) {
        const unsigned cw = 64 * i + lane, j = (cw * 171u) >> 12, cp = cw - 24u * j;
        vw[i] = (8u * (j >> 2) + (j & 3)) * 3072u + 32u * ((cp >> 1) ^ ((j >> 1) & 3)) + 16u * (cp & 1);
      }
      auto issue = [&](int st, unsigned lb) {
        const unsigned sow = st * 196608u + 384u * G + (16u * (lw >> 1) + 4u * (lw & 1)) * 3072u, sox = st * 128u + (unsigned)T0 * 1280u;
#pragma unroll
        for (int i = 0; i < 4; ++i) { const unsigned n = 4 * lw + i; dma16(vx, sx, sox + n * 10240u, lb + M_WB + 1024u * n); }
#pragma unroll
        for (int i = 0; i < 3; ++i) dma16(vw[i], sw, sow, lb + 3072u * lw + 1024u * i);
#pragma unroll
        for (int i = 0; i < 3; ++i) dma16(vw[i], sw, sow + 32u * 3072u, lb + 3072u * (lw + 4) + 1024u * i);
      };
      issue(0, lds0);
      if (lw == 0) dma16c1(vzero, make_srd(p.ws + WS_EPOCH, 16), 0u, lds0 + L_WORDS + 16);
      if (lw == 1) dma16(lane * 16u, make_srd(p.b1, F_ * 2), 384u * G, lds0 + L_BIAS);
      const bool gw_wave = lw == (G >> 1);
      if (gw_wave) { const srd_t sg = make_srd(p.gw, D_ * 2); dma16(lane * 16u, sg, 0u, lds0 + L_GW); dma16(lane * 16u, sg, 1024u, lds0 + L_GW + 1024u); }
      asm volatile("s_waitcnt lgkmcnt(0)" ::: "memory");
      dma4(vzero, sq, 4, f_row);
      LGKM_BAR();
      const unsigned gxa = M_WB + 2048u * G + lane * 16u, gwa = lds0 + L_GW + 16u * ((lane & 7) ^ (lane >> 3));
      float ga0 = 0.f, ga1 = 0.f;
      auto gate_step = [&](const u32x4 x0, const u32x4 x1, const u32x4 gv) {
#pragma unroll
        for (int e = 0; e < 4; ++e) {
          const float gl = __uint_as_float(gv[e] << 16), gh = __uint_as_float(gv[e] & 0xffff0000u);
          ga0 += __uint_as_float(x0[e] << 16) * gl; ga0 += __uint_as_float(x0[e] & 0xffff0000u) * gh;
          ga1 += __uint_as_float(x1[e] << 16) * gl; ga1 += __uint_as_float(x1[e] & 0xffff0000u) * gh;
        }
      };
      unsigned lb = lds0 + M_STAGE;
      for (int st = 1; st < 10; ++st) {
        u32x4 gx0 = {}, gx1 = {}, gv = {};
        if (st >= 3) {
          prog_wait_sleep(f_oth, st - 2);
          if (gw_wave) {
            gx0 = *(LAS u32x4*)(size_t)(lb + gxa); gx1 = *(LAS u32x4*)(size_t)(lb + gxa + 1024u); gv = *(LAS u32x4*)(size_t)(gwa + 128u * (st - 3));
            asm volatile("s_waitcnt lgkmcnt(0)" : "+v"(gx0), "+v"(gx1), "+v"(gv) :: "memory");
          }
        }
        issue(st, lb);
        dma4(vzero, sq, 4 * st + 4, f_row);
        if (gw_wave) gate_step(gx0, gx1, gv);
        lb = lb == lds0 + 2 * M_STAGE ? lds0 : lb + M_STAGE;
      }
      if (gw_wave) {
        __builtin_amdgcn_s_setprio(3);
        for (int st = 7; st < 10; ++st) {
          while (__builtin_amdgcn_readfirstlane(prog_read(f_row)) < (unsigned)(st + 1)) __builtin_amdgcn_s_sleep(1);
          FENCE();
          const u32x4 gx0 = *(LAS u32x4*)(size_t)(lb + gxa), gx1 = *(LAS u32x4*)(size_t)(lb + gxa + 1024u), gv = *(LAS u32x4*)(size_t)(gwa + 128u * st);
          FENCE();
          __hip_atomic_store((LAS unsigned*)(size_t)(lds0 + L_WORDS + 8), st < 9 ? (unsigned)(st + 1) : 63u, RLX_WG);
          FENCE();
          gate_step(gx0, gx1, gv);
          lb = lb == lds0 + 2 * M_STAGE ? lds0 : lb + M_STAGE;
        }
#define DPP_ADD(v, ctrl) v += __uint_as_float(__builtin_amdgcn_update_dpp(0u, __float_as_uint(v), ctrl, 0xf, 0xf, true))
        DPP_ADD(ga0, 0xB1); DPP_ADD(ga1, 0xB1); DPP_ADD(ga0, 0x4E); DPP_ADD(ga1, 0x4E); DPP_ADD(ga0, 0x141); DPP_ADD(ga1, 0x141);
        const float gate = __builtin_amdgcn_rcpf(1.f + __expf(-((lane & 1) ? ga1 : ga0)));
        if ((lane & 6) == 0) *(LAS float*)(size_t)(lds0 + L_GATE + 32u * (lane & 1) + 4u * (lane >> 3)) = gate;
        FENCE();
        const u32x4 gq = *(LAS u32x4*)(size_t)(lds0 + L_GATE + (lane & 3) * 16u);
        if (lane < 4) __builtin_amdgcn_raw_buffer_store_b128(gq, make_srd(p.ws + WS_GATE, S_ * 4), (T0 + 16 * G) * 4 + lane * 16, 0, 16);
        FENCE();
        __builtin_amdgcn_s_setprio(0);
      }
      loader2<8>(p, lds0, lane, lw, rt, 5 * ch + (c & 3), G, f_oth, fidx, gw_wave);
    }
    return;
  }

  const int w = wid - 4;
  if (w == 3) {
    const int l8 = lane & 7, k8 = lane >> 3;
    const char* pa = k8 == 0 ? p.ws + ((rt * 8 + l8) * KS2 + (is_attn ? 2 * G : 20 + 6 * G)) * 1024
                   : k8 == 1 ? (const char*)p.out + (size_t)(T0 + (is_attn ? 32 * (c & 3) + 4 * l8 : 16 * l8)) * 1280 + (5 * ch + (is_attn ? 4 : (c & 3))) * 128
                   : k8 < 5 ? (const char*)p.w2 + (size_t)((k8 - 2) * 8 + l8) * 65536
                   : k8 == 5 ? (const char*)p.wo + (size_t)l8 * 98304
                   : k8 == 6 ? (const char*)p.w2 + (size_t)(24 + (l8 < 6 ? l8 : 0)) * 65536 : (const char*)p.b2;
    touch(pa, lds0 + L_SCR);
  }
  LGKM_BAR();
  if (is_attn) {
    const char* pa; unsigned pstride;
    const int L = (xi * 4 + w) * 36 + (lane < 36 ? lane : 0);
    if (L < 528) { int r = (xcd >> 1) * 512 - 16 + L; r = r < 0 ? 0 : r; pa = (const char*)p.x + r * 1280; pstride = 128u; }
    else if (L < 1488) { const int t = L - 528, k = t / 15, j = t % 15; pa = (const char*)p.wqkv + k * 3840 + (j / 5) * 1280 + ch * 640 + (j % 5) * 128; pstride = 245760u; }
    else { const int t = (L < 2256 ? L : 1488) - 1488, k = t / 12, j = t % 12; pa = (const char*)p.w1 + k * 3072 + ch * 1536 + j * 128; pstride = 196608u; }
    pa += 3 * (size_t)pstride;
#pragma unroll
    for (int st = 3; st < 10; ++st) { touch(pa, lds0 + L_SCR); pa += pstride; }
  }
  __builtin_amdgcn_s_setprio(2);
  const int g = lane >> 4, q = (lane & 15) >> 2, pq = lane & 3, li = lane & 15;
  const unsigned lrow = 16 * (g >> 1) + 4 * (g & 1) + q;
  const srd_t sz = make_srd(p.ws, Z_BYTES);
  const unsigned vz = lane * 16u;
  if (is_attn) {
    unsigned wa[5];
#pragma unroll
    for (int i = 0; i < 4; ++i) wa[i] = lds0 + lrow * 480u + 40u * (i ^ w) + 8u * pq;
    wa[4] = lds0 + lrow * 480u + 40u * pq + 32u;
    unsigned xa[2], xh[2];
#pragma unroll
    for (int ks = 0; ks < 2; ++ks) {
      xh[ks] = lds0 + A_WB + li * 128u + 16u * ((4 * ks + g) ^ (lane & 7));
      xa[ks] = xh[ks] + 2048u * (1 + 2 * w);
    }
    f32x4 acc[15][2], hk0 = {}, hkx = {}, hv0 = {}, hvx = {};
#pragma unroll
    for (int u = 0; u < 15; ++u) { acc[u][0] = f32x4{}; acc[u][1] = f32x4{}; }
    bf16x8 wn[6], xn[3];
    unsigned wc_[5], xc_[2], hc_[2];
#define A_ADR(u, t) (wc_[(u) % 5] + (32 * ((u) / 15) + 8 * (t)) * 480u + (((u) % 15) / 5) * 160u)
#pragma unroll
    for (int i = 0; i < 5; ++i) { wc_[i] = wa[i]; OPAQUE(wc_[i]); }
#pragma unroll
    for (int i = 0; i < 2; ++i) { xc_[i] = xa[i]; hc_[i] = xh[i]; OPAQUE(xc_[i]); OPAQUE(hc_[i]); }
    prog_wait_sleep(f_oth, 1);
    xn[0] = lds_b128(xc_[0]); xn[1] = lds_b128(xc_[0] + 2048u);
#pragma unroll
    for (int i = 0; i < 6; ++i) wn[i] = lds_tr8(A_ADR(i, 0), A_ADR(i, 1));
    xn[2] = lds_b128(hc_[0]);
    SB();
    unsigned sbn = A_STAGE;
    for (int st = 0; st < 10; ++st) {
      bf16x8 wf[30], xf[2][3];
      unsigned seen = 0;
#pragma unroll
      for (int i = 0; i < 6; ++i) wf[i] = wn[i];
#pragma unroll
      for (int i = 0; i < 3; ++i) xf[0][i] = xn[i];
#pragma unroll
      for (int u = 0; u < 30; ++u) {
        const int ks = u / 15, uu = u % 15, sec = uu / 5, tu = (u + 6) % 30;
        if (u == 24) {
          prog_wait(f_oth, seen, st < 9 ? st + 2 : 0);
          SB();
        }
        if (sec < 2) acc[uu][0] = mfma(wf[u], xf[ks][0], acc[uu][0]); else acc[uu][0] = mfma(xf[ks][0], wf[u], acc[uu][0]);
        SB();
        const bf16x4 lo = lds_tr4(A_ADR(tu, 0));
        SB();
        if (sec < 2) acc[uu][1] = mfma(wf[u], xf[ks][1], acc[uu][1]); else acc[uu][1] = mfma(xf[ks][1], wf[u], acc[uu][1]);
        SB();
        const bf16x4 hi = lds_tr4(A_ADR(tu, 1));
        if (u < 24) wf[u + 6] = cat8(lo, hi); else wn[u - 24] = cat8(lo, hi);
        if (u >= 19 && u < 24) { wc_[u - 19] = wa[u - 19] + sbn; OPAQUE(wc_[u - 19]); }
        if (u == 12) { xc_[0] = xa[0] + sbn; OPAQUE(xc_[0]); }
        if (u == 13) { hc_[0] = xh[0] + sbn; OPAQUE(hc_[0]); }
        if (u == 16) { xc_[1] = xa[1] + sbn; OPAQUE(xc_[1]); }
        if (u == 17) { hc_[1] = xh[1] + sbn; OPAQUE(hc_[1]); }
        if (u == 23) prog_set(f_own, st + 1);
        SB();
        if (uu == 5) hk0 = mfma(wf[u], xf[ks][2], hk0);
        if (uu == 9) hkx = mfma(wf[u], xf[ks][2], hkx);
        if (uu == 10) hv0 = mfma(xf[ks][2], wf[u], hv0);
        if (uu == 14) hvx = mfma(xf[ks][2], wf[u], hvx);
        SB();
        if (u == 5) xf[1][0] = lds_b128(xc_[1]);
        if (u == 9) xf[1][1] = lds_b128(xc_[1] + 2048u);
        if (u == 10) xf[1][2] = lds_b128(hc_[1]);
        if (u == 20) seen = prog_read(f_oth);
        if (u == 24) xn[0] = lds_b128(xc_[0]);
        if (u == 25) xn[1] = lds_b128(xc_[0] + 2048u);
        if (u == 29) { xn[2] = lds_b128(hc_[0]); sbn = sbn == 2 * A_STAGE ? 0u : sbn + A_STAGE; }
        SB();
      }
    }
    const unsigned f_cmp = f_oth + 1024u;
    const unsigned seen10 = prog_read(f_cmp);
    u32x4 Kt[4][2];
    u32x2 Vp[5][2];
#pragma unroll
    for (int bk = 1; bk >= 0; --bk) {
#pragma unroll
      for (int i = 0; i < 4; ++i) {
        const u32x2 km = pk4(acc[5 + i][bk]), kx = pk4(acc[9][bk]);
        Kt[i][bk] = u32x4{km[0], km[1], kx[0], kx[1]};
      }
#pragma unroll
      for (int i = 0; i < 5; ++i) Vp[i][bk] = pk4(acc[10 + i][bk]);
      if (bk == 1) {
        prog_wait(f_cmp, seen10, 10);
        const unsigned ek = lds0 + lane * 16u, ev = lds0 + 4096u + lane * 8u;
        if (w < 3) {
#pragma unroll
          for (int i = 0; i < 4; ++i) {
            *(LAS u32x4*)(size_t)(ek + (w + 1) * 8192u + (i ^ w) * 1024u) = Kt[i][1];
            *(LAS u32x2*)(size_t)(ev + (w + 1) * 8192u + (i ^ w) * 512u) = Vp[i][1];
          }
          *(LAS u32x2*)(size_t)(ev + (w + 1) * 8192u + 2048u) = Vp[4][1];
        }
        const u32x2 a = pk4(hk0), ax = pk4(hkx);
        *(LAS u32x4*)(size_t)(ek + w * 1024u) = u32x4{a[0], a[1], ax[0], ax[1]};
        *(LAS u32x2*)(size_t)(ev + w * 512u) = pk4(hv0);
        if (w == 0) *(LAS u32x2*)(size_t)(ev + 2048u) = pk4(hvx);
        prog_set(f_own, 11);
      }
    }
    const int par = li & 1;
    const bool cura = (4 * g + par) <= li, curb = (4 * g + par + 2) <= li;
    const unsigned hm = par ? 0xffff0000u : 0x0000ffffu;
    const unsigned ma0 = cura ? 0u : hm, ma1 = cura ? hm : 0u, mb0 = curb ? 0u : hm, mb1 = curb ? hm : 0u;
    const float SC = 0.22360679774997896f * 1.4426950408889634f, NEG = -1e9f * 1.4426950408889634f;
    const bool first = (rt == 0) && (w == 0);
    float pinf = __builtin_inff(); asm volatile("" : "+s"(pinf));
    unsigned mq[4], mv[4];
#pragma unroll
    for (int i = 0; i < 4; ++i) { mq[i] = (g == (i ^ w)) ? 0xffffffffu : 0u; mv[i] = (q == (i ^ w)) ? 0xffffffffu : 0u; }
    auto attend = [&](const int bk, const u32x4 (&Kprev)[4], const u32x2 (&Vprev)[5]) {
      const bool fm = first && bk == 0;
      const float sca = (fm && !cura) ? 0.f : SC, bia = (fm && !cura) ? NEG : 0.f;
      const float scb = (fm && !curb) ? 0.f : SC, bib = (fm && !curb) ? NEG : 0.f;
      const u32x2 Qx = pk4(acc[4][bk]);
      f32x4 s0[4], s1[4];
#pragma unroll
      for (int i = 0; i < 4; ++i) {
        const u32x2 Qm = pk4(acc[i][bk]);
        const u32x4 Qf = {Qm[0], Qm[1], Qx[0] & mq[i], Qx[1] & mq[i]};
        s0[i] = mfmau(Kprev[i], Qf, f32x4{}); s1[i] = mfmau(Kt[i][bk], Qf, f32x4{});
      }
      u32x4 Vx[4];
#pragma unroll
      for (int i = 0; i < 4; ++i) Vx[i] = u32x4{Vprev[4][0] & mv[i], Vprev[4][1] & mv[i], Vp[4][bk][0] & mv[i], Vp[4][bk][1] & mv[i]};
      float la[4], lb[4], mx[4];
#pragma unroll
      for (int i = 0; i < 4; ++i) {
        float e0 = s0[i][0], e2 = s0[i][2], f0 = s1[i][0], f2 = s1[i][2];
        asm("" : "+v"(e0), "+v"(e2), "+v"(f0), "+v"(f2));
        const float s0a = par ? s0[i][1] : e0, s0b = par ? s0[i][3] : e2;
        const float s1a = par ? s1[i][1] : f0, s1b = par ? s1[i][3] : f2;
        la[i] = (cura ? s1a : s0a) * sca + bia; lb[i] = (curb ? s1b : s0b) * scb + bib;
        mx[i] = vmax(la[i], lb[i]);
      }
      swap32(mx[0], mx[1]); swap32(mx[2], mx[3]);
      float m01 = vmax(mx[0], mx[1]), m23 = vmax(mx[2], mx[3]);
      swap16(m01, m23);
      float mA = vmax(m01, m23), mB = mA;
      swap16(mA, mB);
      mx[0] = mA; mx[1] = mA; mx[2] = mB; mx[3] = mB;
      swap32(mx[0], mx[1]); swap32(mx[2], mx[3]);
      float ea[4], eb[4], sm[4];
#pragma unroll
      for (int i = 0; i < 4; ++i) {
        ea[i] = __builtin_amdgcn_exp2f(la[i] - mx[i]); eb[i] = __builtin_amdgcn_exp2f(lb[i] - mx[i]);
        sm[i] = ea[i] + eb[i];
      }
      swap32(sm[0], sm[1]); swap32(sm[2], sm[3]);
      float t01 = sm[0] + sm[1], t23 = sm[2] + sm[3];
      swap16(t01, t23);
      float iA = __builtin_amdgcn_rcpf(t01 + t23), iB = iA;
      swap16(iA, iB);
      sm[0] = iA; sm[1] = iA; sm[2] = iB; sm[3] = iB;
      swap32(sm[0], sm[1]); swap32(sm[2], sm[3]);
      f32x4 om[4], ox = {};
#pragma unroll
      for (int i = 0; i < 4; ++i) {
        const float pa = ea[i] * sm[i], pb = eb[i] * sm[i];
        const unsigned ta = pk2(pa, pa), tb = pk2(pb, pb);
        const u32x4 P = {ta & ma0, tb & mb0, ta & ma1, tb & mb1};
        om[i] = mfmau(u32x4{Vprev[i][0], Vprev[i][1], Vp[i][bk][0], Vp[i][bk][1]}, P, f32x4{});
        ox = mfmau(Vx[i], P, ox);
      }
      const unsigned zrow = vz + (unsigned)((rt * 8 + 2 * w + bk) * KS2) * 1024u;
      const bool sw = w & 1;
#pragma unroll
      for (int i = 0; i < 4; i += 2) {
        const u32x2 o0 = pk4(om[i]), o1 = pk4(om[i + 1]);
        const u32x4 v = {sw ? o1[0] : o0[0], sw ? o1[1] : o0[1], sw ? o0[0] : o1[0], sw ? o0[1] : o1[1]};
        __builtin_amdgcn_raw_buffer_store_b128(v, sz, zrow + (unsigned)(2 * G + ((i ^ w) >> 1)) * 1024u, 0, 16);
      }
      __builtin_amdgcn_raw_buffer_store_b64(pk4(ox), sz, zrow + (unsigned)(16 + (G >> 1)) * 1024u + 8u * (G & 1), 0, 16);
    };
    {
      const u32x4 Kp0[4] = {Kt[0][0], Kt[1][0], Kt[2][0], Kt[3][0]};
      const u32x2 Vp0[5] = {Vp[0][0], Vp[1][0], Vp[2][0], Vp[3][0], Vp[4][0]};
      attend(1, Kp0, Vp0);
    }
    prog_wait(f_cmp, prog_read(f_cmp), 11);
    u32x4 Kh[4]; u32x2 Vh[5];
#pragma unroll
    for (int i = 0; i < 4; ++i) {
      Kh[i] = *(LAS u32x4*)(size_t)(lds0 + lane * 16u + w * 8192u + (i ^ w) * 1024u);
      Vh[i] = *(LAS u32x2*)(size_t)(lds0 + 4096u + lane * 8u + w * 8192u + (i ^ w) * 512u);
    }
    Vh[4] = *(LAS u32x2*)(size_t)(lds0 + 4096u + lane * 8u + w * 8192u + 2048u);
    prog_set(f_own, 12);
    attend(0, Kh, Vh);
    compute2<2>(p, lds0, lane, w, rt * 8 + 2 * (c & 3), 5 * ch + 4, 0, f_own, fidx);
  } else {
    const int wr = w >> 1, wc = w & 1;
    const unsigned f = 2 * (g & 1) + (q >> 1);
    unsigned wa[6];
#pragma unroll
    for (int n = 0; n < 6; ++n) wa[n] = lds0 + lrow * 384u + 32u * ((6 * wc + n) ^ f) + 8u * pq;
    unsigned xa[2];
#pragma unroll
    for (int ks = 0; ks < 2; ++ks) xa[ks] = lds0 + M_WB + (64 * wr + li) * 128u + 16u * ((4 * ks + g) ^ (lane & 7));
    f32x4 acc[6][4];
    bf16x8 wn[3], xn[4];
    unsigned wc_[6], xc_[2];
#define M_ADR(u, t) (wc_[(u) % 6] + (32 * ((u) / 6) + 8 * (t)) * 384u)
#pragma unroll
    for (int i = 0; i < 6; ++i) { wc_[i] = wa[i]; OPAQUE(wc_[i]); }
#pragma unroll
    for (int i = 0; i < 2; ++i) { xc_[i] = xa[i]; OPAQUE(xc_[i]); }
    prog_wait_sleep(f_oth, 1);
    wn[0] = lds_tr8(M_ADR(0, 0), M_ADR(0, 1));
#pragma unroll
    for (int i = 0; i < 4; ++i) xn[i] = lds_b128(xc_[0] + 2048u * i);
    wn[1] = lds_tr8(M_ADR(1, 0), M_ADR(1, 1)); wn[2] = lds_tr8(M_ADR(2, 0), M_ADR(2, 1));
#pragma unroll
    for (int n = 0; n < 6; ++n) {
      const f32x4 bi = bf4_to_f32(*(LAS u32x2*)(size_t)(lds0 + L_BIAS + (96 * wc + 16 * n + 4 * g) * 2));
#pragma unroll
      for (int m = 0; m < 4; ++m) acc[n][m] = bi;
    }
    SB();
    unsigned sbn = M_STAGE;
    for (int st = 0; st < 10; ++st) {
      bf16x8 wf[12], xf[2][4];
      unsigned seen = 0;
#pragma unroll
      for (int i = 0; i < 3; ++i) wf[i] = wn[i];
#pragma unroll
      for (int i = 0; i < 4; ++i) xf[0][i] = xn[i];
#pragma unroll
      for (int u = 0; u < 12; ++u) {
        const int ks = u / 6, n = u % 6, tu = (u + 3) % 12;
        if (u == 8) {
          prog_wait(f_oth, seen, st < 9 ? st + 2 : 0);
          SB();
        }
        acc[n][0] = mfma(wf[u], xf[ks][0], acc[n][0]);
        SB();
        const bf16x4 lo = lds_tr4(M_ADR(tu, 0));
        SB();
        acc[n][1] = mfma(wf[u], xf[ks][1], acc[n][1]);
        SB();
        const bf16x4 hi = lds_tr4(M_ADR(tu, 1));
        if (u < 9) wf[u + 3] = cat8(lo, hi); else wn[u - 9] = cat8(lo, hi);
        SB();
        acc[n][2] = mfma(wf[u], xf[ks][2], acc[n][2]);
        SB();
        if (u < 4) xf[1][u] = lds_b128(xc_[1] + 2048u * u);
        if (u == 5) seen = prog_read(f_oth);
        if (u >= 8) xn[u - 8] = lds_b128(xc_[0] + 2048u * (u - 8));
        SB();
        acc[n][3] = mfma(wf[u], xf[ks][3], acc[n][3]);
        SB();
        if (u >= 3 && u < 9) { wc_[u - 3] = wa[u - 3] + sbn; OPAQUE(wc_[u - 3]); }
        if (u == 1) { xc_[0] = xa[0] + sbn; OPAQUE(xc_[0]); }
        if (u == 9) { xc_[1] = xa[1] + sbn; OPAQUE(xc_[1]); }
        if (u == 8) prog_set(f_own, st + 1);
        if (u == 11) sbn = sbn == 2 * M_STAGE ? 0u : sbn + M_STAGE;
        SB();
      }
    }
    prog_set(f_own, 12);
    const float C1 = -2.f * 0.7978845608028654f * 1.4426950408889634f, C2 = C1 * 0.044715f;
#pragma unroll
    for (int m = 0; m < 4; ++m) {
      const unsigned zrow = vz + (unsigned)((rt * 8 + 4 * wr + m) * KS2 + 20 + 6 * G + 3 * wc) * 1024u;
#pragma unroll
      for (int j = 0; j < 3; ++j) {
        u32x4 o;
#pragma unroll
        for (int e = 0; e < 2; ++e) {
#pragma unroll
          for (int k = 0; k < 2; ++k) {
            const f32x2 x = {acc[2 * j + e][m][2 * k], acc[2 * j + e][m][2 * k + 1]};
            const f32x2 t = x * (x * x * C2 + C1);
            f32x2 d = {__builtin_amdgcn_exp2f(t[0]), __builtin_amdgcn_exp2f(t[1])};
            d = d + 1.f;
            f32x2 r = {__builtin_amdgcn_rcpf(d[0]), __builtin_amdgcn_rcpf(d[1])};
            r = r * x;
            o[2 * e + k] = pk2(r[0], r[1]);
          }
        }
        __builtin_amdgcn_raw_buffer_store_b128(o, sz, zrow + 1024u * j, 0, 16);
      }
    }
    compute2<8>(p, lds0, lane, w, rt * 8, 5 * ch + (c & 3), G, f_own, fidx);
  }
}

extern "C" void kernel_launch(void* const* d_in, const int* in_sizes, int n_in, void* d_out, int out_size, void* d_ws,
                              size_t ws_size, hipStream_t stream) {
  (void)in_sizes; (void)n_in; (void)out_size; (void)ws_size;
  (void)hipFuncSetAttribute((const void*)fused, hipFuncAttributeMaxDynamicSharedMemorySize, L_TOTAL);
  Args a{d_in[0], d_in[1], d_in[4], d_in[5], d_in[8], d_in[2], d_in[6], d_in[7], d_out, (char*)d_ws};
  fused<<<256, 512, L_TOTAL, stream>>>(a);
}
```
